```python
import torch

def _softmax(x, axis=-1):
    return torch.softmax(x, dim=axis)
def _gelu(x):
    return torch.nn.functional.gelu(x, approximate="tanh")
def _silu(x):
    return torch.nn.functional.silu(x)
def _sigmoid(x):
    return torch.sigmoid(x)
def _layernorm(x, axis=-1, eps=1e-5):
    m = x.mean(dim=axis, keepdim=True)
    v = x.var(dim=axis, keepdim=True, unbiased=False)
    return (x - m) / torch.sqrt(v + eps)
def _scatter_add(tgt, idx, src, axis=0):
    return tgt.clone().index_add_(axis, idx, src)
def _segment_sum(src, seg, n):
    out = torch.zeros((n,) + src.shape[1:], dtype=src.dtype)
    return out.index_add_(0, seg, src)
def _pad(x, pw):
    flat = tuple(v for pair in reversed(pw) for v in pair)
    return torch.nn.functional.pad(x, flat)

kernel_name = 'concat_heads__gqa_block_cached__mha_block_cached__near_ridge'

def setup_inputs():

    _w = (lambda a: (lambda t: t.to(torch.bfloat16) if t.is_floating_point() else t)(torch.as_tensor(a)))
    def _A_setup():
        B, Sq, D = 16, 144, 640
        Hq, Hkv, Sk, Dh = 32, 32, 512, 192
        assert Hq % Hkv == 0, (Hq, Hkv)
        return {
            "x": _w((lambda *s: torch.randn(*s, dtype=torch.bfloat16) * 0.8)(B, Sq, D)),
            "k_cache": _w((lambda *s: torch.randn(*s, dtype=torch.bfloat16) * 0.8)(B, Hkv, Sk, Dh)),
            "v_cache": _w((lambda *s: torch.randn(*s, dtype=torch.bfloat16) * 0.05)(B, Hkv, Sk, Dh)),
            "wq": _w((lambda *s: torch.randn(*s, dtype=torch.bfloat16) * 0.8)(D, Hq * Dh) * (D ** -0.5)),
            "wo": _w((lambda *s: torch.randn(*s, dtype=torch.bfloat16) * 0.05)(Hq * Dh, D) * ((Hq * Dh) ** -0.5)),
        }
    _A_in = _A_setup()

    def _B_setup():
        B, Sq, D = 16, 144, 640
        H, Sk, Dh = 32, 512, 192
        return {
            "x": _w((lambda *s: torch.randn(*s, dtype=torch.bfloat16) * 0.8)(B, Sq, D)),
            "k_cache": _w((lambda *s: torch.randn(*s, dtype=torch.bfloat16) * 0.8)(B, H, Sk, Dh)),
            "v_cache": _w((lambda *s: torch.randn(*s, dtype=torch.bfloat16) * 0.05)(B, H, Sk, Dh)),
            "wq": _w((lambda *s: torch.randn(*s, dtype=torch.bfloat16) * 0.8)(D, H * Dh) * (D ** -0.5)),
            "wo": _w((lambda *s: torch.randn(*s, dtype=torch.bfloat16) * 0.05)(H * Dh, D) * ((H * Dh) ** -0.5)),
        }
    _B_in = _B_setup()

    return {
        "A_x": _A_in["x"],
        "A_k_cache": _A_in["k_cache"],
        "A_v_cache": _A_in["v_cache"],
        "A_wq": _A_in["wq"],
        "A_wo": _A_in["wo"],
        "B_x": _B_in["x"],
        "B_k_cache": _B_in["k_cache"],
        "B_v_cache": _B_in["v_cache"],
        "B_wq": _B_in["wq"],
        "B_wo": _B_in["wo"],
    }

def reference(A_x, A_k_cache, A_v_cache, A_wq, A_wo, B_x, B_k_cache, B_v_cache, B_wq, B_wo):
    def _A_ref(x, k_cache, v_cache, wq, wo):
        B, Sq, D = x.shape
        Hq, Dh = 32, 192
        Hkv = k_cache.shape[1]
        rep = Hq // Hkv
        q = (lambda x, *d: x.permute(*d))((x @ wq).reshape(B, Sq, Hq, Dh), 0, 2, 1, 3)
        k = (lambda x, n, axis: torch.repeat_interleave(x, n, dim=axis))(k_cache, rep, axis=1)
        v = (lambda x, n, axis: torch.repeat_interleave(x, n, dim=axis))(v_cache, rep, axis=1)
        scores = torch.einsum("bhqd,bhkd->bhqk", q, k) * (Dh ** -0.5)
        p = _softmax(scores, axis=-1)
        o = torch.einsum("bhqk,bhkd->bhqd", p, v)
        return (lambda x, *d: x.permute(*d))(o, 0, 2, 1, 3).reshape(B, Sq, Hq * Dh) @ wo

    def _B_ref(x, k_cache, v_cache, wq, wo):
        B, Sq, D = x.shape
        H, Dh = 32, 192
        q = (lambda x, *d: x.permute(*d))((x @ wq).reshape(B, Sq, H, Dh), 0, 2, 1, 3)
        scores = torch.einsum("bhqd,bhkd->bhqk", q, k_cache) * (Dh ** -0.5)
        p = _softmax(scores, axis=-1)
        o = torch.einsum("bhqk,bhkd->bhqd", p, v_cache)
        return (lambda x, *d: x.permute(*d))(o, 0, 2, 1, 3).reshape(B, Sq, H * Dh) @ wo

    a = _A_ref(A_x, A_k_cache, A_v_cache, A_wq, A_wo)
    b = _B_ref(B_x, B_k_cache, B_v_cache, B_wq, B_wo)
    return torch.concatenate([a, b], axis=-1)
```

```cpp
#include <hip/hip_runtime.h>
#include <hip/hip_bf16.h>
#include <stdint.h>
#include <utility>

typedef __bf16   bf16x8 __attribute__((ext_vector_type(8)));
typedef __bf16   bf16x4 __attribute__((ext_vector_type(4)));
typedef __bf16   bf16x2 __attribute__((ext_vector_type(2)));
typedef float    f32x4  __attribute__((ext_vector_type(4)));
typedef float    f32x2  __attribute__((ext_vector_type(2)));
typedef unsigned u32x4  __attribute__((ext_vector_type(4)));
typedef unsigned u32x2  __attribute__((ext_vector_type(2)));
#define LAS __attribute__((address_space(3)))
#define DI __device__ __forceinline__
template <int... Is> using iseq = std::integer_sequence<int, Is...>;
template <int N> using make_iseq = std::make_integer_sequence<int, N>;

constexpr int NB = 16, SQ = 144, DM = 640, NH = 32, SK = 512, DH = 192;
constexpr int HD = NH * DH;
constexpr unsigned SLOT = 24576;
constexpr unsigned SCR_OFF = 6 * SLOT;
constexpr unsigned FLAG_OFF = SCR_OFF + 3 * 5120;
constexpr unsigned LDS1_BYTES = FLAG_OFF + 64;
constexpr float QSCALE = 0.07216878364870322f * 1.4426950408889634f;
constexpr float THR = 8.0f;
constexpr float THR_LO = 32.0f;

constexpr size_t WS_O_BYTES  = (size_t)2 * NB * SQ * HD * 2;
constexpr size_t WS_WO_OFF   = WS_O_BYTES;

#define GAP() __builtin_amdgcn_sched_barrier(0)
#define OPAQUE(x) asm volatile("" : "+v"(x))

#define ACLOB "a0","a1","a2","a3","a4","a5","a6","a7","a8","a9","a10","a11","a12","a13","a14","a15","a16","a17","a18","a19","a20","a21","a22","a23","a24","a25","a26","a27","a28","a29","a30","a31","a32","a33","a34","a35","a36","a37","a38","a39","a40","a41","a42","a43","a44","a45","a46","a47","a48","a49","a50","a51","a52","a53","a54","a55","a56","a57","a58","a59","a60","a61","a62","a63","a64","a65","a66","a67","a68","a69","a70","a71","a72","a73","a74","a75","a76","a77","a78","a79","a80","a81","a82","a83","a84","a85","a86","a87","a88","a89","a90","a91","a92","a93","a94","a95","a96","a97","a98","a99","a100","a101","a102","a103","a104","a105","a106","a107","a108","a109","a110","a111","a112","a113","a114","a115","a116","a117","a118","a119","a120","a121","a122","a123","a124","a125","a126","a127","a128","a129","a130","a131","a132","a133","a134","a135","a136","a137","a138","a139","a140","a141","a142","a143","a144","a145","a146","a147","a148","a149","a150","a151","a152","a153","a154","a155"

DI unsigned cvtpk(float lo, float hi) {
  f32x2 v = {lo, hi}; bf16x2 b = __builtin_convertvector(v, bf16x2); return __builtin_bit_cast(unsigned, b);
}
DI float max3f(float a, float b, float c) { float r; asm("v_max3_f32 %0, %1, %2, %3" : "=v"(r) : "v"(a), "v"(b), "v"(c)); return r; }
DI float min3f(float a, float b, float c) { float r; asm("v_min3_f32 %0, %1, %2, %3" : "=v"(r) : "v"(a), "v"(b), "v"(c)); return r; }
DI float max2f(float a, float b) { float r; asm("v_max_f32_e32 %0, %1, %2" : "=v"(r) : "v"(a), "v"(b)); return r; }
DI float fsub_s(float a, float b) { float r; asm("v_sub_f32_e32 %0, %1, %2" : "=v"(r) : "v"(a), "v"(b)); return r; }
DI bf16x8 lds_b128(unsigned addr) { return *(const LAS bf16x8*)(uintptr_t)addr; }
DI bf16x4 lds_tr(unsigned a) { return __builtin_amdgcn_ds_read_tr16_b64_v4bf16((LAS bf16x4*)(uintptr_t)a); }
DI bf16x8 cat4(bf16x4 lo, bf16x4 hi) { return __builtin_shufflevector(lo, hi, 0, 1, 2, 3, 4, 5, 6, 7); }
DI float xmax16_32(float v) {
  auto r = __builtin_amdgcn_permlane16_swap(__float_as_uint(v), __float_as_uint(v), false, false);
  v = fmaxf(__uint_as_float(r[0]), __uint_as_float(r[1]));
  auto s = __builtin_amdgcn_permlane32_swap(__float_as_uint(v), __float_as_uint(v), false, false);
  return fmaxf(__uint_as_float(s[0]), __uint_as_float(s[1]));
}

constexpr int AO(int rt, int t) { return 4 * (13 * rt + t); }
template <int R, bool Z> DI void mfma_acc(const bf16x8& a, const bf16x8& b) {
  if constexpr (Z) asm volatile("v_mfma_f32_16x16x32_bf16 a[%c2:%c3], %0, %1, 0" :: "v"(a), "v"(b), "i"(R), "i"(R + 3));
  else             asm volatile("v_mfma_f32_16x16x32_bf16 a[%c2:%c3], %0, %1, a[%c2:%c3]" :: "v"(a), "v"(b), "i"(R), "i"(R + 3));
}
template <int R> DI float acc_read() { float r; asm volatile("v_accvgpr_read_b32 %0, a[%c1]" : "=v"(r) : "i"(R)); return r; }
template <int R> DI void acc_scale4(float f) {
  float t0, t1;
  asm volatile("v_accvgpr_read_b32 %0, a[%c3]\n\tv_accvgpr_read_b32 %1, a[%c4]\n\tv_mul_f32 %0, %2, %0\n\tv_mul_f32 %1, %2, %1\n\t"
               "v_accvgpr_write_b32 a[%c3], %0\n\tv_accvgpr_write_b32 a[%c4], %1\n\t"
               "v_accvgpr_read_b32 %0, a[%c5]\n\tv_accvgpr_read_b32 %1, a[%c6]\n\tv_mul_f32 %0, %2, %0\n\tv_mul_f32 %1, %2, %1\n\t"
               "v_accvgpr_write_b32 a[%c5], %0\n\tv_accvgpr_write_b32 a[%c6], %1"
               : "=&v"(t0), "=&v"(t1) : "v"(f), "i"(R), "i"(R + 1), "i"(R + 2), "i"(R + 3));
}

struct St {
  unsigned aX0, aX1, aK0, aK1, aW[4], aV[4];
  unsigned bX0, bX1, bW[4], bK0, bK1, bV[4];
  f32x4 qa[3][12];
  bf16x8 qf[3][6];
  f32x4 S[2][3][2];
  u32x4 P[2][3];
  bf16x4 flo[4], fhi[4];
  bf16x8 fk[4];
  bf16x8 xf[2][3];
  f32x4 negm4[3];
  float mx[3], fct[3];
  bf16x8 ones;
  bool pend;
  unsigned aflag, adone, flagv, step;
  float inv[3], et[8];
  u32x4 ew;
  __amdgpu_buffer_rsrc_t rO;
  unsigned voO;
};

#define STAMP(i)
enum { PH_QP = 0, PH_QK = 1, PH_PV = 2 };

template <int PH, int HALF, int F, int PART> DI void read_frag(St& c) {
  if constexpr (PH == PH_QP) {
    constexpr int ks = F / 12, t = F % 12;
    const unsigned a = c.bW[t & 3] + (t >> 2) * 128 + ks * (32 * 384);
    if constexpr (PART == 0) c.flo[F & 3] = lds_tr(a); else c.fhi[F & 3] = lds_tr(a + 4 * 384);
  } else if constexpr (PH == PH_QK) {
    constexpr int kt = HALF * 2 + F / 6, s = F % 6;
    if constexpr (PART == 0) c.fk[F & 3] = lds_b128(((s & 1) ? c.bK1 : c.bK0) + kt * 6144 + (s >> 1) * 128);
  } else {
    const unsigned a = c.bV[F & 3] + (F >> 2) * 128 + HALF * (32 * 384);
    if constexpr (PART == 0) c.flo[F & 3] = lds_tr(a); else c.fhi[F & 3] = lds_tr(a + 16 * 384);
  }
}
template <int KS, int RT> DI void read_x(St& c) {
  c.xf[KS][RT] = lds_b128((KS ? c.bX1 : c.bX0) + RT * 2048); }
template <unsigned C> DI unsigned vadd_c(unsigned v) { unsigned r; asm volatile("v_add_u32 %0, %1, %2" : "=v"(r) : "i"(C), "v"(v)); return r; }
DI void flag_read(St& c) { c.flagv = *(volatile LAS unsigned*)(uintptr_t)c.aflag; }
DI void flag_wait(St& c, unsigned need) {
  if (__builtin_expect(__builtin_amdgcn_readfirstlane(c.flagv) < need, 0)) {
    unsigned tv, ts;
    asm volatile("1:\n\tds_read_b32 %0, %2\n\ts_waitcnt lgkmcnt(0)\n\tv_readfirstlane_b32 %1, %0\n\ts_cmp_lt_u32 %1, %3\n\ts_cbranch_scc1 1b"
                 : "=&v"(tv), "=&s"(ts) : "v"(c.aflag), "s"(need) : "memory", "scc");
  }
  asm volatile("" ::: "memory");
}
DI void done_signal(St& c) {
  asm volatile("" ::: "memory");
  c.step = vadd_c<1>(c.step);
  *(volatile LAS unsigned*)(uintptr_t)c.adone = c.step;
}

constexpr int SM_NOPS = 13 + 36;
template <int X, bool FIRST, int I> DI void sm_op(St& c) {
  if constexpr (I < 12) {
    constexpr int kt2 = I / 6, rt = (I % 6) % 3, st = (I % 6) / 3;
    const f32x4& s = c.S[X][rt][kt2];
    if constexpr (kt2 == 0 && st == 0) c.mx[rt] = max3f(s[0], s[1], s[2]);
    else if constexpr (kt2 == 0)       c.mx[rt] = max2f(c.mx[rt], s[3]);
    else if constexpr (st == 0)        c.mx[rt] = max3f(c.mx[rt], s[0], s[1]);
    else                               c.mx[rt] = max3f(c.mx[rt], s[2], s[3]);
  } else if constexpr (I == 12) {
    bool bad = max3f(c.mx[0], c.mx[1], c.mx[2]) > THR;
    if constexpr (FIRST) bad = bad || (min3f(c.mx[0], c.mx[1], c.mx[2]) < -THR_LO);
    if (__builtin_expect(__any((int)bad), 0)) {
#pragma unroll
      for (int rt = 0; rt < 3; ++rt) {
        float d = xmax16_32(c.mx[rt]);
        if constexpr (!FIRST) d = fmaxf(d, 0.f);
        const float nm = c.negm4[rt][0] - d;
        c.negm4[rt] = f32x4{nm, nm, nm, nm}; OPAQUE(c.negm4[rt]);
#pragma unroll
        for (int kt2 = 0; kt2 < 2; ++kt2)
#pragma unroll
          for (int r = 0; r < 4; ++r) c.S[X][rt][kt2][r] = fsub_s(c.S[X][rt][kt2][r], d);
        c.fct[rt] = __builtin_amdgcn_exp2f(-d);
      }
      if constexpr (!FIRST) c.pend = true;
    }
  } else {
    constexpr int J = I - 13, rt = J / 12, k = J % 12;
    if constexpr (k < 8) {
      float e = __builtin_amdgcn_exp2f(c.S[X][rt][k >> 2][k & 3]); OPAQUE(e);
      c.S[X][rt][k >> 2][k & 3] = e;
    } else {
      constexpr int p = k - 8;
      unsigned w = cvtpk(c.S[X][rt][p >> 1][2 * (p & 1)], c.S[X][rt][p >> 1][2 * (p & 1) + 1]); OPAQUE(w);
      c.P[X][rt][p] = w;
    }
  }
}
template <int X, bool FIRST, int LO, int... Is> DI void sm_ops_(St& c, iseq<Is...>) { (sm_op<X, FIRST, LO + Is>(c), ...); }
template <int X, bool FIRST, int LO, int HI> DI void sm_ops(St& c) { if constexpr (HI > LO) sm_ops_<X, FIRST, LO>(c, make_iseq<HI - LO>{}); }

constexpr int sm_cost(int i) { return i < 12 ? 4 : (i == 12 ? 8 : (((i - 13) % 12) < 8 ? 8 : 4)); }
struct SmSched { int op[75][2]; int last; int pv_done[SM_NOPS]; };
constexpr SmSched make_sched() {
  SmSched s{};
  int at[SM_NOPS] = {};
  for (int i = 0; i < SM_NOPS; ++i) at[i] = -1;
  s.last = 0;
  for (int gi = 0; gi < 75; ++gi) {
    s.op[gi][0] = -1; s.op[gi][1] = -1;
    const bool pv = gi < 39;
    const int m = pv ? gi : gi - 39, grp = m / 3, r = m % 3;
    int cap = 8;
    if (pv) { if (grp < 9) { if (r < 2) cap = 4; } else if (grp < 12) { if (r == 0) cap = 4; } }
    else {
      if (grp < 9) { if (r == 0) cap = 4; } else { if (r < 2) cap = 4; }
      if (grp < 4 && r == 2) cap = 4;
      if (grp == 6 && r == 1) cap = 4;
      if (grp == 8 && r >= 1) cap = 0;
      if ((grp == 9 || grp == 10) && r == 2) cap = 4;
    }
    int n = 0;
    for (int pass = 0; pass < 2; ++pass)
      for (int i = 0; i < SM_NOPS && n < 2; ++i) {
        if (at[i] >= 0) continue;
        const int cst = sm_cost(i);
        if ((pass == 0) != (cst == 8) || cst > cap) continue;
        bool ok = true;
        if (i < 12) { if (i >= 3) ok = at[i - 3] >= 0 && at[i - 3] < gi; if (i >= 6 && gi < 4) ok = false; }
        else if (i == 12) { for (int k = 0; k < 12; ++k) ok = ok && at[k] >= 0 && at[k] < gi; }
        else {
          const int J = i - 13, rt = J / 12, k = J % 12;
          if (k < 8) ok = at[12] >= 0 && at[12] < gi;
          else { const int e0 = 13 + rt * 12 + 2 * (k - 8); ok = at[e0] >= 0 && at[e0] < gi && at[e0 + 1] >= 0 && at[e0 + 1] < gi; }
        }
        if (!ok) continue;
        at[i] = gi; s.op[gi][n++] = i; cap -= cst; s.last = gi;
      }
  }
  for (int i = 0; i < SM_NOPS; ++i) { s.pv_done[i] = (at[i] >= 0 && at[i] < 39) ? 1 : 0; if (at[i] < 0) s.last = 1000; }
  return s;
}
constexpr SmSched SCHED_N = make_sched();
static_assert(SCHED_N.last <= 72, "softmax schedule must finish before the last gaps of the QK phase");
template <int X, int... Is> DI void sm_rest_(St& c, iseq<Is...>) { ((SCHED_N.pv_done[Is] ? (void)0 : sm_op<X, false, Is>(c)), ...); }

template <int... Is> DI void rescale_(St& c, iseq<Is...>) { (acc_scale4<4 * Is>(c.fct[Is / 13]), ...); }
DI void rescale(St& c) {
  asm volatile("s_nop 7" ::: "memory");
  rescale_(c, make_iseq<39>{});
  asm volatile("s_nop 1" ::: "memory");
}

constexpr int EP_NOPS(int es) { return (es == 0 ? 6 : 0) + 63; }
template <int ES, int I0> DI void ep_op(St& c) {
  if constexpr (ES == 0 && I0 < 6) {
    constexpr int rt = I0 % 3;
    if constexpr (I0 < 3) c.inv[rt] = acc_read<AO(rt, 12)>();
    else { c.inv[rt] = __builtin_amdgcn_rcpf(c.inv[rt]); OPAQUE(c.inv[rt]); }
  } else {
    constexpr int I = I0 - (ES == 0 ? 6 : 0), rt = I / 21, k = I % 21;
    if constexpr (k < 8) c.et[k] = acc_read<AO(rt, 2 * ES + (k >> 2)) + (k & 3)>();
    else if constexpr (k < 16) { c.et[k - 8] = c.et[k - 8] * c.inv[rt]; OPAQUE(c.et[k - 8]); }
    else if constexpr (k < 20) { unsigned w = cvtpk(c.et[2 * (k - 16)], c.et[2 * (k - 16) + 1]); OPAQUE(w); c.ew[k - 16] = w; }
    else __builtin_amdgcn_raw_buffer_store_b128(c.ew, c.rO, c.voO, (ES * 9 + rt) * 1024, 0);
  }
}
template <int ES, int LO, int... Is> DI void ep_ops_(St& c, iseq<Is...>) { (ep_op<ES, LO + Is>(c), ...); }
template <int ES, int LO, int HI> DI void ep_ops(St& c) {
  constexpr int H2 = HI > EP_NOPS(ES) ? EP_NOPS(ES) : HI;
  if constexpr (H2 > LO) ep_ops_<ES, LO>(c, make_iseq<H2 - LO>{});
}

constexpr int ep_slot(int M) {
  int n = 0;
  for (int m = 0; m < M; ++m) {
    const int g = m / 3, r = m % 3;
    int cap = (r < 2) ? 1 : 2;
    if (r == 2 && g >= 6 && g < 9) cap = 1;
    if (r == 2 && g == 10) cap = 0;
    if (r == 2 && g == 16) cap = 1;
    if (g >= 17) cap = (r < 2 && g < 20) ? 1 : ((r == 2 && g == 18) ? 2 : 0);
    n += cap;
  }
  return n;
}
static_assert(ep_slot(72) >= 69, "epilogue ops must fit");
template <int PH, int HALF, int X, int NPH, int NHALF, int NL0, int NL1, int ND, bool Z, int SM, int ES, int M>
DI void gap(St& c, unsigned nb) {
  constexpr bool SYNC = NL0 >= 0;
  constexpr int NGF = (PH == PH_QP) ? 24 : 12;
  constexpr int g = M / 3, rt = M % 3;
  constexpr unsigned S0 = SYNC ? (unsigned)(NL0 % 6) * SLOT : 0u, S1 = (SYNC && NL1 >= 0) ? (unsigned)(NL1 % 6) * SLOT : 0u;
  if constexpr (PH == PH_QP) {
    constexpr int ks = g / 12, t = g % 12;
    const bf16x8 f = cat4(c.flo[g & 3], c.fhi[g & 3]);
    if constexpr (Z && ks == 0) asm volatile("v_mfma_f32_16x16x32_bf16 %0, %1, %2, 0" : "=&v"(c.qa[rt][t]) : "v"(f), "v"(c.xf[ks][rt]));
    else                        asm volatile("v_mfma_f32_16x16x32_bf16 %0, %1, %2, %0" : "+v"(c.qa[rt][t]) : "v"(f), "v"(c.xf[ks][rt]));
  } else if constexpr (PH == PH_QK) {
    constexpr int kt2 = g / 6, s = g % 6;
    if constexpr (s == 0) asm volatile("v_mfma_f32_16x16x32_bf16 %0, %1, %2, %3" : "=&v"(c.S[X][rt][kt2]) : "v"(c.fk[g & 3]), "v"(c.qf[rt][s]), "v"(c.negm4[rt]));
    else                  asm volatile("v_mfma_f32_16x16x32_bf16 %0, %1, %2, %0" : "+v"(c.S[X][rt][kt2]) : "v"(c.fk[g & 3]), "v"(c.qf[rt][s]));
  } else {
    const bf16x8 pf = __builtin_bit_cast(bf16x8, c.P[X][rt]);
    if constexpr (g < 12) mfma_acc<AO(rt, g), Z>(cat4(c.flo[g & 3], c.fhi[g & 3]), pf);
    else                  mfma_acc<AO(rt, 12), Z>(c.ones, pf);
  }
  GAP();
  if constexpr (PH == PH_QK && M == 24) asm volatile("" :: "v"(c.negm4[0]), "v"(c.negm4[1]), "v"(c.negm4[2]));
  if constexpr (rt < 2) {
    if constexpr (g + 3 < NGF) read_frag<PH, HALF, g + 3, rt>(c);
    else if constexpr (g + 3 - NGF < 3) read_frag<NPH, NHALF, g + 3 - NGF, rt>(c);
  }
  if constexpr (PH == PH_QP && g >= 6 && g < 9 && rt == 2) read_x<1, g - 6>(c);
  if constexpr (NPH == PH_QP && g == NGF - 3 && rt == 2) { read_x<0, 0>(c); read_x<0, 1>(c); }
  if constexpr (NPH == PH_QP && g == NGF - 2 && rt == 2) read_x<0, 2>(c);
  if constexpr (SYNC) {
    if constexpr (PH == PH_QK ? (g == 6 && rt == 1) : (g == NGF - 8 && rt == 2)) flag_read(c);
    if constexpr (PH == PH_QK) {
      if constexpr (g == 8 && rt == 1) flag_wait(c, nb + ND);
      if constexpr (g == 8 && rt == 2) done_signal(c);
      if constexpr (g < 4 && rt == 2) c.bV[g] = vadd_c<S1>(c.aV[g]);
      if constexpr (g == 9 && rt == 2) c.bK0 = vadd_c<S0>(c.aK0);
      if constexpr (g == 10 && rt == 2) c.bK1 = vadd_c<S0>(c.aK1);
    } else {
      if constexpr (g == NGF - 5 && rt == 2) flag_wait(c, nb + ND);
      if constexpr (g == NGF - 4 && rt == 2) done_signal(c);
      if constexpr (NPH == PH_QK) {
        if constexpr (g == 10 && rt == 2) { c.bK0 = vadd_c<S0>(c.aK0); c.bK1 = vadd_c<S0>(c.aK1); }
      } else if constexpr (PH == PH_QP) {
        if constexpr (g == 10 && rt == 2) { c.bX0 = vadd_c<S0>(c.aX0); c.bX1 = vadd_c<S0>(c.aX1); }
        if constexpr (g == 17 && rt == 2) c.bW[0] = vadd_c<S1>(c.aW[0]);
        if constexpr (g == 20 && rt == 0) c.bW[1] = vadd_c<S1>(c.aW[1]);
        if constexpr (g == 20 && rt == 1) c.bW[2] = vadd_c<S1>(c.aW[2]);
        if constexpr (g == 22 && rt == 2) c.bW[3] = vadd_c<S1>(c.aW[3]);
      } else {
        if constexpr (g == 0 && rt == 2) { c.bX0 = vadd_c<S0>(c.aX0); c.bX1 = vadd_c<S0>(c.aX1); }
        if constexpr (g == 1 && rt == 2) { c.bW[0] = vadd_c<S1>(c.aW[0]); c.bW[1] = vadd_c<S1>(c.aW[1]); }
        if constexpr (g == 2 && rt == 2) { c.bW[2] = vadd_c<S1>(c.aW[2]); c.bW[3] = vadd_c<S1>(c.aW[3]); }
      }
    }
  }
  if constexpr (SM == 1) {
    constexpr int gi = (PH == PH_PV) ? M : 39 + M;
    if constexpr (SCHED_N.op[gi][0] >= 0) sm_op<X ^ 1, false, SCHED_N.op[gi][0]>(c);
    if constexpr (SCHED_N.op[gi][1] >= 0) sm_op<X ^ 1, false, SCHED_N.op[gi][1]>(c);
  } else if constexpr (SM == 2) {
    if constexpr (M >= 24 && M < 30) sm_ops<X, true, M - 24, M - 23>(c);
  } else if constexpr (SM == 3) {
    if constexpr (M < 34) sm_ops<X ^ 1, true, 13 + M, 14 + M>(c);
    if constexpr (M == 33) sm_ops<X ^ 1, true, 13 + 34, 13 + 36>(c);
  }
  if constexpr (ES >= 0) {
    constexpr int e0 = ep_slot(M), e1 = ep_slot(M + 1);
    ep_ops<ES, e0, e1>(c);
  }
  GAP();
}
template <int PH, int HALF, int X, int NPH, int NHALF, int NL0, int NL1, int ND, bool Z, int SM, int ES, int... Ms>
DI void phase_(St& c, unsigned nb, iseq<Ms...>) { (gap<PH, HALF, X, NPH, NHALF, NL0, NL1, ND, Z, SM, ES, Ms>(c, nb), ...); }
template <int PH, int HALF, int X, int NPH, int NHALF, int NL0, int NL1, int ND, bool Z, int SM, int ES = -1>
DI void phase(St& c, unsigned nb) {
  constexpr int NM = (PH == PH_QP) ? 72 : (PH == PH_QK ? 36 : 39);
  phase_<PH, HALF, X, NPH, NHALF, NL0, NL1, ND, Z, SM, ES>(c, nb, make_iseq<NM>{});
  if constexpr (PH == PH_PV && SM == 1) {
    if (__builtin_expect(c.pend, 0)) { rescale(c); c.pend = false; }
    GAP();
  }
}

template <int RT, int S> DI void q_convert(St& c) {
  const f32x4 a = c.qa[RT][2 * S] * QSCALE, b = c.qa[RT][2 * S + 1] * QSCALE;
  unsigned pa0 = cvtpk(a[0], a[1]), pa1 = cvtpk(a[2], a[3]);
  unsigned pb0 = cvtpk(b[0], b[1]), pb1 = cvtpk(b[2], b[3]);
  auto r0 = __builtin_amdgcn_permlane32_swap(pa0, pb0, false, false);
  auto r1 = __builtin_amdgcn_permlane32_swap(pa1, pb1, false, false);
  auto t0 = __builtin_amdgcn_permlane16_swap(r0[0], r0[1], false, false);
  auto t1 = __builtin_amdgcn_permlane16_swap(r1[0], r1[1], false, false);
  u32x4 w = {t0[0], t1[0], t0[1], t1[1]};
  c.qf[RT][S] = __builtin_bit_cast(bf16x8, w);
}
template <int... Is> DI void q_convert_all(St& c, iseq<Is...>) { (q_convert<Is % 3, Is / 3>(c), ...); }
template <int... Es> DI void ep_all(St& c, iseq<Es...>) { (ep_ops<Es, 0, 100>(c), ...); }

__host__ __device__ constexpr unsigned slot_of(int n) { return (unsigned)(n % 6) * SLOT; }
__host__ __device__ constexpr int kload(int j) { return j == 0 ? 20 : 2 * j + 19; }
__host__ __device__ constexpr int vload(int j) { return j == 7 ? 35 : 2 * j + 22; }

template <int J> DI void att_step(St& c, unsigned nb) {
  phase<PH_PV, 0, 0, PH_QK, 0, -1, -1, 0, J == 0, 1>(c, nb);
  phase<PH_QK, 0, 0, PH_PV, 1, -1, -1, 0, false, 1>(c, nb);
  phase<PH_PV, 1, 1, PH_QK, 1, -1, -1, 0, false, 1>(c, nb);
  phase<PH_QK, 1, 1, PH_PV, 0, (J < 6 ? kload(J + 2) : 0), vload(J + 1), (J < 6 ? 2 * J + 25 : 36), false, 1>(c, nb);
}
template <int... Js> DI void att_steps(St& c, unsigned nb, iseq<Js...>) { (att_step<Js>(c, nb), ...); }

struct Ld {
  __amdgpu_buffer_rsrc_t rX, rK, rV, rW;
  unsigned vK[6], vV[3], vW[12], vX[4];
  unsigned ldsb, sW, sX[2], sK[2];
  unsigned base_step, base_load;
  bool more, first;
};
#define DMA16I(rsrc, ldsoff, voff, soff, imm, aux) \
  __builtin_amdgcn_raw_ptr_buffer_load_lds((rsrc), (LAS void*)(uintptr_t)(ldsoff), 16, (voff), (soff), (imm), (aux))
constexpr int c_hi(int T) { return T < 0 ? (T == -1 ? 0 : -1) : (T < 10 ? 2 * T + 2 : (T == 10 ? 21 : (T < 18 ? 2 * T + 1 : (T == 18 ? 36 : 38)))); }
constexpr int n_instr1(int N) { const int n = N % 36; return (n < 20 && (n & 1) == 0) ? 18 : 24; }
constexpr int n_instr(int lo, int hi) { int r = 0; for (int i = lo; i < hi; ++i) r += n_instr1(i); return r; }
template <int N, int P> DI void issue_piece(Ld& ld) {
  constexpr int n = N % 36, nx = N / 36;
  const unsigned l0 = ld.ldsb + slot_of(n) + (P >> 2) * 4096;
  constexpr int imm = (P & 3) * 1024;
  if constexpr (n < 20 && (n & 1) == 0) {
    DMA16I(ld.rX, l0, ld.vX[P & 3], ld.sX[nx] + (n >> 1) * 128 + (P >> 2) * (32 * DM * 2), imm, 0);
  } else if constexpr (n < 20) {
    DMA16I(ld.rW, l0, ld.vW[P % 12], ld.sW + (unsigned)((n >> 1) * 64 + (P / 12) * 32) * (HD * 2), imm, 0);
  } else {
    constexpr bool isK = (n == 20) || (n < 35 && (n & 1));
    constexpr int j = isK ? (n == 20 ? 0 : (n - 19) >> 1) : (n == 35 ? 7 : (n - 22) >> 1);
    if constexpr (isK) DMA16I(ld.rK, l0, ld.vK[P % 6], ld.sK[nx] + j * SLOT + (P >> 2) * 4096, imm, 2);
    else               DMA16I(ld.rV, l0, ld.vV[P % 3], ld.sK[nx] + j * SLOT + (P >> 2) * 4096, imm, 2);
  }
}
template <int N, int... Ps> DI void issue_load_(Ld& ld, iseq<Ps...>) { (issue_piece<N, Ps>(ld), ...); }
template <int N> DI void issue_load(Ld& ld) {
  if constexpr (N >= 36) { if (ld.more) issue_load_<N>(ld, make_iseq<n_instr1(N)>{}); }
  else issue_load_<N>(ld, make_iseq<n_instr1(N)>{});
}
template <int LO, int... Is> DI void issue_range_(Ld& ld, iseq<Is...>) { (issue_load<LO + Is>(ld), ...); }
template <int LO, int HI> DI void issue_range(Ld& ld) { issue_range_<LO>(ld, make_iseq<HI - LO>{}); }
template <int N> DI void vm_wait() { asm volatile("s_waitcnt vmcnt(%0)" :: "i"(N > 63 ? 63 : N) : "memory"); }
DI void publish(Ld& ld, unsigned v) {
  asm volatile("ds_write_b32 %0, %1" :: "v"(ld.ldsb + FLAG_OFF), "v"(v) : "memory");
}
DI void wait_done(Ld& ld, unsigned need) {
  while (true) {
    u32x4 d;
    asm volatile("ds_read_b128 %0, %1\n\ts_waitcnt lgkmcnt(0)" : "=v"(d) : "v"(ld.ldsb + FLAG_OFF + 16) : "memory");
    const unsigned m = __builtin_amdgcn_readfirstlane(min(min(d[0], d[1]), d[2]));
    if (m >= need) break;
    __builtin_amdgcn_s_sleep(1);
  }
}
template <int T> DI void loader_step(Ld& ld) {
  constexpr int lo = c_hi(T - 2) + 6, hi = c_hi(T - 1) + 6, need = c_hi(T + 1);
  wait_done(ld, ld.base_step + T);
  issue_range<lo, hi>(ld);
  if constexpr (hi > 36) {
    if (ld.more) vm_wait<n_instr(need, hi)>();
    else vm_wait<(need < 36 ? n_instr(need, 36) : 0)>();
  } else vm_wait<n_instr(need, hi)>();
  publish(ld, ld.base_load + need);
}
template <int... Ts> DI void loader_unit(Ld& ld, iseq<Ts...>) { (loader_step<Ts>(ld), ...); }

__global__ __launch_bounds__(256, 1)
void attn_fused_kernel(const uint16_t* __restrict__ xA, const uint16_t* __restrict__ kA, const uint16_t* __restrict__ vA,
                       const uint16_t* __restrict__ wqA, const uint16_t* __restrict__ woA,
                       const uint16_t* __restrict__ xB, const uint16_t* __restrict__ kB, const uint16_t* __restrict__ vB,
                       const uint16_t* __restrict__ wqB, const uint16_t* __restrict__ woB,
                       uint16_t* __restrict__ Ows, uint16_t* __restrict__ WOws) {
  extern __shared__ __attribute__((aligned(16))) char smem[];
  const unsigned ldsb = (unsigned)(uintptr_t)(LAS char*)smem;
  const int tid = threadIdx.x;
  const int wave = __builtin_amdgcn_readfirstlane(tid >> 6);
  const int l = tid & 63;
  const int wg = blockIdx.x;
  const int xcd = wg & 7, wi = wg >> 3;
  const int blk = xcd >> 2;
  const int head = (xcd & 3) * 8 + (wi & 7);
  const int bsub = wi >> 3;

  if (tid < 16) *(volatile LAS unsigned*)(uintptr_t)(ldsb + FLAG_OFF + tid * 4) = 0u;
  asm volatile("s_waitcnt lgkmcnt(0)\n\ts_barrier" ::: "memory");

  if (wave == 3) {
    const uint16_t* xp = blk ? xB : xA; const uint16_t* kp = blk ? kB : kA;
    const uint16_t* vp = blk ? vB : vA; const uint16_t* wqp = blk ? wqB : wqA;
    Ld ld;
    ld.rX = __builtin_amdgcn_make_buffer_rsrc((void*)xp, 0, NB * SQ * DM * 2, 0x00020000);
    ld.rK = __builtin_amdgcn_make_buffer_rsrc((void*)kp, 0, NB * NH * SK * DH * 2, 0x00020000);
    ld.rV = __builtin_amdgcn_make_buffer_rsrc((void*)vp, 0, NB * NH * SK * DH * 2, 0x00020000);
    ld.rW = __builtin_amdgcn_make_buffer_rsrc((void*)wqp, 0, DM * HD * 2, 0x00020000);
    ld.ldsb = ldsb;
    const unsigned l3 = l >> 3, l7 = l & 7;
#pragma unroll
    for (int p = 0; p < 12; ++p) {
      const unsigned line = 8 * p + l3, row = line / 3, seg = line % 3;
      const unsigned f = (((row >> 1) & 1) | ((row >> 2) & 2)) << 1;
      ld.vW[p] = row * (HD * 2) + seg * 128 + ((l7 ^ f) << 4) - (p & 3) * 1024;
      if (p < 6) ld.vK[p] = l3 * 128 + ((l7 ^ ((row >> 1) & 7)) << 4);
      if (p < 3) ld.vV[p] = l3 * 128 + ((l7 ^ (((row >> 1) & 3) << 1)) << 4);
      if (p < 4) ld.vX[p] = line * (DM * 2) + ((l7 ^ ((line >> 1) & 7)) << 4) - p * 1024;
    }
    ld.sW = head * (DH * 2);
    ld.sX[0] = (unsigned)(bsub * SQ) * (DM * 2);
    ld.sK[0] = (unsigned)((bsub * NH + head) * SK) * (DH * 2);
    ld.more = true; ld.first = true;
    issue_range<0, 5>(ld);
    vm_wait<n_instr(2, 5)>();
    publish(ld, 2);
    for (int u = 0; u < 4; ++u) {
      ld.more = u < 3;
      ld.base_step = 19 * u; ld.base_load = 36 * u;
      ld.sX[1] = ld.sX[0] + 4 * SQ * (DM * 2);
      ld.sK[1] = ld.sK[0] + 4 * NH * SK * (DH * 2);
      loader_unit(ld, make_iseq<19>{});
      ld.sX[0] = ld.sX[1]; ld.sK[0] = ld.sK[1]; ld.first = false;
    }
    return;
  }

  const int cc = l & 15, g = l >> 4, q4 = (l >> 2) & 3, p4 = l & 3;
  {
    const int rb = wg >> 1, nh = wg & 1;
    const int b2 = rb >> 6, ks = (rb & 63) * 3 + wave;
    const uint16_t* wo = (b2 ? woB : woA) + (size_t)(32 * ks + (l >> 3)) * DM + nh * 320 + (l & 7) * 8;
    u32x4 w[5][4];
#pragma unroll
    for (int ch = 0; ch < 5; ++ch)
#pragma unroll
      for (int i = 0; i < 4; ++i) w[ch][i] = *(const u32x4*)(wo + (size_t)(8 * i) * DM + ch * 64);
    const unsigned scr = ldsb + SCR_OFF + wave * 5120;
    const unsigned wa = scr + (l >> 3) * 160 + (l & 7) * 16;
    const unsigned ra = scr + (4 * g + q4) * 160 + p4 * 8;
    uint16_t* dstw = WOws + ((size_t)((b2 * 8 + nh * 4) * 192 + ks) * 5) * 512 + l * 8;
#pragma unroll
    for (int ch = 0; ch < 5; ++ch) {
#pragma unroll
      for (int i = 0; i < 4; ++i) *(LAS u32x4*)(uintptr_t)(wa + i * 8 * 160) = w[ch][i];
#pragma unroll
      for (int t = 0; t < 4; ++t) {
        const bf16x8 f = cat4(lds_tr(ra + t * 32), lds_tr(ra + t * 32 + 16 * 160));
        *(bf16x8*)(dstw + ((size_t)((ch * 4 + t) / 5) * 192 * 5 + (ch * 4 + t) % 5) * 512) = f;
      }
    }
  }
  St c;
  {
    const unsigned swz8 = (cc >> 1) & 7;
    c.aX0 = ldsb + (48 * wave + cc) * 128 + ((g ^ swz8) << 4);
    c.aX1 = ldsb + (48 * wave + cc) * 128 + (((4 + g) ^ swz8) << 4);
    c.aK0 = ldsb + cc * 384 + ((g ^ swz8) << 4);
    c.aK1 = ldsb + cc * 384 + (((4 + g) ^ swz8) << 4);
    const unsigned fp = ((q4 >> 1) & 1) | ((g & 1) << 1);
#pragma unroll
    for (int i = 0; i < 4; ++i) {
      const unsigned inrow = ((i ^ fp) << 5) + ((p4 >> 1) << 4) + ((p4 & 1) << 3);
      c.aW[i] = ldsb + (8 * g + q4) * 384 + inrow;
      c.aV[i] = ldsb + (4 * g + q4) * 384 + inrow;
    }
    u32x4 o1 = {0x3F803F80u, 0x3F803F80u, 0x3F803F80u, 0x3F803F80u};
    c.ones = __builtin_bit_cast(bf16x8, o1); OPAQUE(c.ones);
    c.pend = false;
    c.aflag = ldsb + FLAG_OFF; c.adone = ldsb + FLAG_OFF + 16 + wave * 4; c.step = 0;
    OPAQUE(c.aflag); OPAQUE(c.adone); OPAQUE(c.step);
    c.voO = l * 16;
    c.rO = __builtin_amdgcn_make_buffer_rsrc((void*)Ows, 0, 0, 0x00020000);
    asm volatile("" ::: ACLOB);
  }

  flag_read(c); flag_wait(c, 2);
  c.bX0 = vadd_c<slot_of(0)>(c.aX0); c.bX1 = vadd_c<slot_of(0)>(c.aX1);
  c.bW[0] = vadd_c<slot_of(1)>(c.aW[0]); c.bW[1] = vadd_c<slot_of(1)>(c.aW[1]); c.bW[2] = vadd_c<slot_of(1)>(c.aW[2]); c.bW[3] = vadd_c<slot_of(1)>(c.aW[3]);
  read_frag<PH_QP, 0, 0, 0>(c); read_frag<PH_QP, 0, 0, 1>(c);
  read_x<0, 0>(c); read_x<0, 1>(c); read_x<0, 2>(c);
  read_frag<PH_QP, 0, 1, 0>(c); read_frag<PH_QP, 0, 1, 1>(c);
  read_frag<PH_QP, 0, 2, 0>(c); read_frag<PH_QP, 0, 2, 1>(c);

  for (int u = 0; u < 4; ++u) {
    const int batch = 4 * u + bsub;
    const unsigned nb = 36 * u;
    GAP();
    STAMP(0);
    phase<PH_QP, 0, 0, PH_QP, 0,  2,  3,  4, true,  0, 0>(c, nb);
    phase<PH_QP, 0, 0, PH_QP, 0,  4,  5,  6, false, 0, 1>(c, nb);
    phase<PH_QP, 0, 0, PH_QP, 0,  6,  7,  8, false, 0, 2>(c, nb);
    phase<PH_QP, 0, 0, PH_QP, 0,  8,  9, 10, false, 0, 3>(c, nb);
    phase<PH_QP, 0, 0, PH_QP, 0, 10, 11, 12, false, 0, 4>(c, nb);
    phase<PH_QP, 0, 0, PH_QP, 0, 12, 13, 14, false, 0, 5>(c, nb);
    STAMP(1);
    phase<PH_QP, 0, 0, PH_QP, 0, 14, 15, 16, false, 0>(c, nb);
    phase<PH_QP, 0, 0, PH_QP, 0, 16, 17, 18, false, 0>(c, nb);
    phase<PH_QP, 0, 0, PH_QP, 0, 18, 19, 20, false, 0>(c, nb);
    STAMP(2);
    phase<PH_QP, 0, 0, PH_QK, 0, kload(0), -1, 21, false, 0>(c, nb);
    STAMP(10);
    asm volatile("s_nop 7" ::: "memory");
    GAP();
    q_convert_all(c, make_iseq<18>{});
#pragma unroll
    for (int rt = 0; rt < 3; ++rt) { c.negm4[rt] = f32x4{0.f, 0.f, 0.f, 0.f}; OPAQUE(c.negm4[rt]); }
    GAP();
    asm volatile("s_nop 1" ::: "memory");
    GAP();
    STAMP(11);
    phase<PH_QK, 0, 0, PH_QK, 1, -1, -1, 0, false, 2>(c, nb);
    asm volatile("s_nop 7" ::: "memory");
    GAP();
    sm_ops<0, true, 6, 13>(c);
    GAP();
    asm volatile("s_nop 1" ::: "memory");
    GAP();
    phase<PH_QK, 1, 1, PH_PV, 0, kload(1), vload(0), 23, false, 3>(c, nb);
    att_steps(c, nb, make_iseq<7>{});
    phase<PH_PV, 0, 0, PH_PV, 1, -1, -1, 0, false, 1>(c, nb);
    sm_rest_<1>(c, make_iseq<SM_NOPS>{});
    GAP();
    asm volatile("s_nop 1" ::: "memory");
    GAP();
    phase<PH_PV, 1, 1, PH_QP, 0, 0, 1, 38, false, 0>(c, nb);
    STAMP(39);
    c.rO = __builtin_amdgcn_make_buffer_rsrc((void*)(Ows + ((size_t)((blk * NB + batch) * 192 + head * 6) * 9 + wave * 3) * 512), 0, 48 * 1024, 0x00020000);
    asm volatile("s_nop 7" ::: "memory");
    STAMP(40);
  }
  GAP();
  ep_all(c, make_iseq<6>{});
}

constexpr unsigned LDS2_BYTES = 45 * 3 * 1024;
struct Op {
  u32x4 of[4][9], wf[4][5];
  __amdgpu_buffer_rsrc_t rO, rW;
  unsigned vo[9];
};
template <int ST, int I> DI void op_load(Op& o, unsigned koff) {
  if constexpr (I < 5) o.wf[ST][I] = __builtin_amdgcn_raw_buffer_load_b128(o.rW, o.vo[I], koff * 5, 0);
  else                 o.of[ST][I - 5] = __builtin_amdgcn_raw_buffer_load_b128(o.rO, o.vo[I - 5], koff * 9, 0);
}
template <int ST, bool Z, int M> DI void op_gap(Op& o, unsigned koff_next) {
  constexpr int rt = M / 5, nt = M % 5, R = 4 * M;
  if constexpr (Z) asm volatile("v_mfma_f32_16x16x32_bf16 a[%c2:%c3], %0, %1, 0" :: "v"(o.wf[ST][nt]), "v"(o.of[ST][rt]), "i"(R), "i"(R + 3));
  else             asm volatile("v_mfma_f32_16x16x32_bf16 a[%c2:%c3], %0, %1, a[%c2:%c3]" :: "v"(o.wf[ST][nt]), "v"(o.of[ST][rt]), "i"(R), "i"(R + 3));
  GAP();
  if constexpr (M % 3 == 1 && M / 3 < 14) op_load<(ST + 3) % 4, M / 3>(o, koff_next);
  GAP();
}
template <int ST, bool Z, int... Ms> DI void op_step_(Op& o, unsigned koff_next, iseq<Ms...>) { (op_gap<ST, Z, Ms>(o, koff_next), ...); }
template <int ST, bool Z> DI void op_step(Op& o, unsigned koff_next) { op_step_<ST, Z>(o, koff_next, make_iseq<45>{}); }
template <int ST, int... Is> DI void op_load_all(Op& o, unsigned koff, iseq<Is...>) { (op_load<ST, Is>(o, koff), ...); }
template <int I> DI f32x4 acc_read4() { f32x4 v; v[0] = acc_read<4 * I>(); v[1] = acc_read<4 * I + 1>(); v[2] = acc_read<4 * I + 2>(); v[3] = acc_read<4 * I + 3>(); return v; }
template <int I> DI void op_red_write(f32x4* red, int wave, int l) {
  constexpr int own = I & 3;
  if (own != wave) { const int slot = (wave < own) ? wave : wave - 1; red[(I * 3 + slot) * 64 + l] = acc_read4<I>(); }
}
template <int I> DI void op_red_final(const f32x4* red, int wave, int l, uint16_t* outp) {
  if ((I & 3) == wave) {
    f32x4 v = acc_read4<I>();
    v += red[(I * 3 + 0) * 64 + l]; v += red[(I * 3 + 1) * 64 + l]; v += red[(I * 3 + 2) * 64 + l];
    constexpr int rt = I / 5, nt = I % 5;
    u32x2 w = {cvtpk(v[0], v[1]), cvtpk(v[2], v[3])};
    *(u32x2*)(outp + (size_t)(rt * 16) * (2 * DM) + nt * 16) = w;
  }
}
template <int... Is> DI void op_red_write_all(f32x4* red, int wave, int l, iseq<Is...>) { (op_red_write<Is>(red, wave, l), ...); }
template <int... Is> DI void op_red_final_all(const f32x4* red, int wave, int l, uint16_t* outp, iseq<Is...>) { (op_red_final<Is>(red, wave, l, outp), ...); }

__global__ __launch_bounds__(256, 1)
void oproj_kernel(const uint16_t* __restrict__ Ows, const uint16_t* __restrict__ WOws, uint16_t* __restrict__ out) {
  extern __shared__ __attribute__((aligned(16))) char smem[];
  const int tid = threadIdx.x;
  const int wave = __builtin_amdgcn_readfirstlane(tid >> 6);
  const int l = tid & 63, c = l & 15, g = l >> 4;
  const int wg = blockIdx.x, xcd = wg & 7, wi = wg >> 3;
  const int blk = xcd >> 2, batch = (xcd & 3) * 4 + (wi >> 3), ng = wi & 7;
  Op o;
  o.rO = __builtin_amdgcn_make_buffer_rsrc((void*)(Ows + ((size_t)((blk * NB + batch) * 192 + wave * 48) * 9) * 512), 0, 48 * 9 * 1024, 0x00020000);
  o.rW = __builtin_amdgcn_make_buffer_rsrc((void*)(WOws + ((size_t)((blk * 8 + ng) * 192 + wave * 48) * 5) * 512), 0, 48 * 5 * 1024, 0x00020000);
#pragma unroll
  for (int i = 0; i < 9; ++i) { o.vo[i] = l * 16 + i * 1024; OPAQUE(o.vo[i]); }
  asm volatile("" ::: ACLOB, "a156","a157","a158","a159","a160","a161","a162","a163","a164","a165","a166","a167","a168","a169","a170","a171","a172","a173","a174","a175","a176","a177","a178","a179");
  op_load_all<0>(o, 0, make_iseq<14>{});
  op_load_all<1>(o, 1024, make_iseq<14>{});
  op_load_all<2>(o, 2048, make_iseq<14>{});
  GAP();
  op_step<0, true>(o, 3 * 1024);
  op_step<1, false>(o, 4 * 1024);
  op_step<2, false>(o, 5 * 1024);
  op_step<3, false>(o, 6 * 1024);
  for (int ks = 4; ks < 48; ks += 4) {
    const unsigned k3 = (ks + 3) * 1024, k4 = (ks + 4 < 48 ? ks + 4 : 47) * 1024, k5 = (ks + 5 < 48 ? ks + 5 : 47) * 1024, k6 = (ks + 6 < 48 ? ks + 6 : 47) * 1024;
    op_step<0, false>(o, k3);
    op_step<1, false>(o, k4);
    op_step<2, false>(o, k5);
    op_step<3, false>(o, k6);
  }
  asm volatile("s_nop 7" ::: "memory");
  GAP();
  f32x4* red = (f32x4*)smem;
  op_red_write_all(red, wave, l, make_iseq<45>{});
  __syncthreads();
  uint16_t* outp = out + (size_t)(batch * SQ + c) * (2 * DM) + blk * DM + ng * 80 + 4 * g;
  op_red_final_all(red, wave, l, outp, make_iseq<45>{});
}

extern "C" void kernel_launch(void* const* d_in, const int* in_sizes, int n_in,
                              void* d_out, int out_size, void* d_ws, size_t ws_size,
                              hipStream_t stream) {
  (void)in_sizes; (void)n_in; (void)out_size; (void)ws_size;
  const uint16_t* xA = (const uint16_t*)d_in[0]; const uint16_t* kA = (const uint16_t*)d_in[1];
  const uint16_t* vA = (const uint16_t*)d_in[2]; const uint16_t* wqA = (const uint16_t*)d_in[3];
  const uint16_t* woA = (const uint16_t*)d_in[4];
  const uint16_t* xB = (const uint16_t*)d_in[5]; const uint16_t* kB = (const uint16_t*)d_in[6];
  const uint16_t* vB = (const uint16_t*)d_in[7]; const uint16_t* wqB = (const uint16_t*)d_in[8];
  const uint16_t* woB = (const uint16_t*)d_in[9];
  uint16_t* Ows = (uint16_t*)d_ws;
  uint16_t* WOws = (uint16_t*)((char*)d_ws + WS_WO_OFF);
  (void)hipFuncSetAttribute((const void*)attn_fused_kernel, hipFuncAttributeMaxDynamicSharedMemorySize, (int)LDS1_BYTES);
  (void)hipFuncSetAttribute((const void*)oproj_kernel, hipFuncAttributeMaxDynamicSharedMemorySize, (int)LDS2_BYTES);
  hipLaunchKernelGGL(attn_fused_kernel, dim3(256), dim3(256), LDS1_BYTES, stream,
                     xA, kA, vA, wqA, woA, xB, kB, vB, wqB, woB, Ows, WOws);
  hipLaunchKernelGGL(oproj_kernel, dim3(256), dim3(256), LDS2_BYTES, stream, Ows, WOws, (uint16_t*)d_out);
}
```

```cpp
#include <hip/hip_runtime.h>
#include <hip/hip_bf16.h>
#include <stdint.h>
#include <utility>

typedef __bf16   bf16x8 __attribute__((ext_vector_type(8)));
typedef __bf16   bf16x4 __attribute__((ext_vector_type(4)));
typedef __bf16   bf16x2 __attribute__((ext_vector_type(2)));
typedef float    f32x4  __attribute__((ext_vector_type(4)));
typedef float    f32x2  __attribute__((ext_vector_type(2)));
typedef unsigned u32x4  __attribute__((ext_vector_type(4)));
typedef unsigned u32x2  __attribute__((ext_vector_type(2)));
#define LAS __attribute__((address_space(3)))
#define DI __device__ __forceinline__
template <int... Is> using iseq = std::integer_sequence<int, Is...>;
template <int N> using make_iseq = std::make_integer_sequence<int, N>;

constexpr int NB = 16, SQ = 144, DM = 640, NH = 32, SK = 512, DH = 192;
constexpr int HD = NH * DH;
constexpr unsigned SLOT = 24576;
constexpr unsigned SCR_OFF = 6 * SLOT;
constexpr unsigned FLAG_OFF = SCR_OFF + 5120;
constexpr unsigned LDS1_BYTES = FLAG_OFF + 64;
constexpr float QSCALE = 0.07216878364870322f * 1.4426950408889634f;
constexpr float THR = 8.0f;
constexpr float THR_LO = 32.0f;

constexpr size_t WS_O_BYTES  = (size_t)2 * NB * SQ * HD * 2;
constexpr size_t WS_WO_OFF   = WS_O_BYTES;

#define KV_AUX 2
#define O_AUX 0
#define GAP() __builtin_amdgcn_sched_barrier(0)
#define OPAQUE(x) asm volatile("" : "+v"(x))

#define ACLOB "a0","a1","a2","a3","a4","a5","a6","a7","a8","a9","a10","a11","a12","a13","a14","a15","a16","a17","a18","a19","a20","a21","a22","a23","a24","a25","a26","a27","a28","a29","a30","a31","a32","a33","a34","a35","a36","a37","a38","a39","a40","a41","a42","a43","a44","a45","a46","a47","a48","a49","a50","a51","a52","a53","a54","a55","a56","a57","a58","a59","a60","a61","a62","a63","a64","a65","a66","a67","a68","a69","a70","a71","a72","a73","a74","a75","a76","a77","a78","a79","a80","a81","a82","a83","a84","a85","a86","a87","a88","a89","a90","a91","a92","a93","a94","a95","a96","a97","a98","a99","a100","a101","a102","a103","a104","a105","a106","a107","a108","a109","a110","a111","a112","a113","a114","a115","a116","a117","a118","a119","a120","a121","a122","a123","a124","a125","a126","a127","a128","a129","a130","a131","a132","a133","a134","a135","a136","a137","a138","a139","a140","a141","a142","a143","a144","a145","a146","a147","a148","a149","a150","a151","a152","a153","a154","a155"

#define ACLOB2 "a156","a157","a158","a159","a160","a161","a162","a163","a164","a165","a166","a167","a168","a169","a170","a171","a172","a173","a174","a175","a176","a177","a178","a179","a180","a181","a182","a183","a184","a185","a186","a187","a188","a189","a190","a191","a192","a193","a194","a195","a196","a197","a198","a199","a200","a201","a202","a203","a204","a205","a206","a207","a208","a209","a210","a211","a212","a213","a214","a215","a216","a217","a218","a219","a220","a221","a222","a223","a224","a225","a226","a227"

DI unsigned cvtpk(float lo, float hi) {
  f32x2 v = {lo, hi}; bf16x2 b = __builtin_convertvector(v, bf16x2); return __builtin_bit_cast(unsigned, b);
}
DI float max3f(float a, float b, float c) { float r; asm("v_max3_f32 %0, %1, %2, %3" : "=v"(r) : "v"(a), "v"(b), "v"(c)); return r; }
DI float min3f(float a, float b, float c) { float r; asm("v_min3_f32 %0, %1, %2, %3" : "=v"(r) : "v"(a), "v"(b), "v"(c)); return r; }
DI float max2f(float a, float b) { float r; asm("v_max_f32_e32 %0, %1, %2" : "=v"(r) : "v"(a), "v"(b)); return r; }
DI float fsub_s(float a, float b) { float r; asm("v_sub_f32_e32 %0, %1, %2" : "=v"(r) : "v"(a), "v"(b)); return r; }
DI bf16x8 lds_b128(unsigned addr) { return *(const LAS bf16x8*)(uintptr_t)addr; }
DI bf16x4 lds_tr(unsigned a) { return __builtin_amdgcn_ds_read_tr16_b64_v4bf16((LAS bf16x4*)(uintptr_t)a); }
DI bf16x8 cat4(bf16x4 lo, bf16x4 hi) { return __builtin_shufflevector(lo, hi, 0, 1, 2, 3, 4, 5, 6, 7); }
DI float xmax16_32(float v) {
  auto r = __builtin_amdgcn_permlane16_swap(__float_as_uint(v), __float_as_uint(v), false, false);
  v = fmaxf(__uint_as_float(r[0]), __uint_as_float(r[1]));
  auto s = __builtin_amdgcn_permlane32_swap(__float_as_uint(v), __float_as_uint(v), false, false);
  return fmaxf(__uint_as_float(s[0]), __uint_as_float(s[1]));
}

constexpr int AO(int rt, int t) { return 4 * (13 * rt + t); }
template <int R, bool Z> DI void mfma_acc(const bf16x8& a, const bf16x8& b) {
  if constexpr (Z) asm volatile("v_mfma_f32_16x16x32_bf16 a[%c2:%c3], %0, %1, 0" :: "v"(a), "v"(b), "i"(R), "i"(R + 3));
  else             asm volatile("v_mfma_f32_16x16x32_bf16 a[%c2:%c3], %0, %1, a[%c2:%c3]" :: "v"(a), "v"(b), "i"(R), "i"(R + 3));
}
template <int R> DI float acc_read() { float r; asm volatile("v_accvgpr_read_b32 %0, a[%c1]" : "=v"(r) : "i"(R)); return r; }
template <int R> DI void acc_scale4(float f) {
  float t0, t1;
  asm volatile("v_accvgpr_read_b32 %0, a[%c3]\n\tv_accvgpr_read_b32 %1, a[%c4]\n\tv_mul_f32 %0, %2, %0\n\tv_mul_f32 %1, %2, %1\n\t"
               "v_accvgpr_write_b32 a[%c3], %0\n\tv_accvgpr_write_b32 a[%c4], %1\n\t"
               "v_accvgpr_read_b32 %0, a[%c5]\n\tv_accvgpr_read_b32 %1, a[%c6]\n\tv_mul_f32 %0, %2, %0\n\tv_mul_f32 %1, %2, %1\n\t"
               "v_accvgpr_write_b32 a[%c5], %0\n\tv_accvgpr_write_b32 a[%c6], %1"
               : "=&v"(t0), "=&v"(t1) : "v"(f), "i"(R), "i"(R + 1), "i"(R + 2), "i"(R + 3));
}

struct St {
  unsigned aX0, aX1, aK0, aK1, aW[4], aV[4];
  unsigned bX0, bX1, bW[4], bK0, bK1, bV[4];
  f32x4 qa[3][12];
  bf16x8 qf[3][6];
  f32x4 S[2][3][2];
  u32x4 P[2][3];
  bf16x4 flo[4], fhi[4];
  bf16x8 fk[4];
  bf16x8 xf[2][3];
  f32x4 negm4[3];
  float mx[3], fct[3];
  bf16x8 ones;
  bool pend;
  unsigned aflag, adone, flagv, step, rs0, rs1;
  float inv[3], et[8];
  u32x4 ew;
  __amdgpu_buffer_rsrc_t rO;
  unsigned voO;
};

#define STAMP(i)
enum { PH_QP = 0, PH_QK = 1, PH_PV = 2 };

template <int PH, int HALF, int F, int PART> DI void read_frag(St& c) {
  if constexpr (PH == PH_QP) {
    constexpr int ks = F / 12, t = F % 12;
    const unsigned a = c.bW[t & 3] + (t >> 2) * 128 + ks * (32 * 384);
    if constexpr (PART == 0) c.flo[F & 3] = lds_tr(a); else c.fhi[F & 3] = lds_tr(a + 4 * 384);
  } else if constexpr (PH == PH_QK) {
    constexpr int kt = HALF * 2 + F / 6, s = F % 6;
    if constexpr (PART == 0) c.fk[F & 3] = lds_b128(((s & 1) ? c.bK1 : c.bK0) + kt * 6144 + (s >> 1) * 128);
  } else {
    const unsigned a = c.bV[F & 3] + (F >> 2) * 128 + HALF * (32 * 384);
    if constexpr (PART == 0) c.flo[F & 3] = lds_tr(a); else c.fhi[F & 3] = lds_tr(a + 16 * 384);
  }
}
template <int KS, int RT> DI void read_x(St& c) {
  c.xf[KS][RT] = lds_b128((KS ? c.bX1 : c.bX0) + RT * 2048); }
DI unsigned vadd_s(unsigned sv, unsigned v) { unsigned r; asm volatile("v_add_u32 %0, %1, %2" : "=v"(r) : "s"(sv), "v"(v)); return r; }
template <unsigned C> DI unsigned vadd_c(unsigned v) { unsigned r; asm volatile("v_add_u32 %0, %1, %2" : "=v"(r) : "i"(C), "v"(v)); return r; }
DI void flag_read(St& c) { c.flagv = *(volatile LAS unsigned*)(uintptr_t)c.aflag; }
DI void flag_wait(St& c, unsigned need) {
  if (__builtin_expect(__builtin_amdgcn_readfirstlane(c.flagv) < need, 0)) {
    unsigned tv, ts;
    asm volatile("1:\n\tds_read_b32 %0, %2\n\ts_waitcnt lgkmcnt(0)\n\tv_readfirstlane_b32 %1, %0\n\ts_cmp_lt_u32 %1, %3\n\ts_cbranch_scc1 1b"
                 : "=&v"(tv), "=&s"(ts) : "v"(c.aflag), "s"(need) : "memory", "scc");
  }
  asm volatile("" ::: "memory");
}
DI void done_signal(St& c) {
  asm volatile("" ::: "memory");
  c.step = vadd_c<1>(c.step);
  *(volatile LAS unsigned*)(uintptr_t)c.adone = c.step;
}

constexpr int SM_NOPS = 13 + 36;
template <int X, bool FIRST, int I> DI void sm_op(St& c) {
  if constexpr (I < 12) {
    constexpr int kt2 = I / 6, rt = (I % 6) % 3, st = (I % 6) / 3;
    const f32x4& s = c.S[X][rt][kt2];
    if constexpr (kt2 == 0 && st == 0) c.mx[rt] = max3f(s[0], s[1], s[2]);
    else if constexpr (kt2 == 0)       c.mx[rt] = max2f(c.mx[rt], s[3]);
    else if constexpr (st == 0)        c.mx[rt] = max3f(c.mx[rt], s[0], s[1]);
    else                               c.mx[rt] = max3f(c.mx[rt], s[2], s[3]);
  } else if constexpr (I == 12) {
    bool bad = max3f(c.mx[0], c.mx[1], c.mx[2]) > THR;
    if constexpr (FIRST) bad = bad || (min3f(c.mx[0], c.mx[1], c.mx[2]) < -THR_LO);
    if (__builtin_expect(__any((int)bad), 0)) {
#pragma unroll
      for (int rt = 0; rt < 3; ++rt) {
        float d = xmax16_32(c.mx[rt]);
        if constexpr (!FIRST) d = fmaxf(d, 0.f);
        const float nm = c.negm4[rt][0] - d;
        c.negm4[rt] = f32x4{nm, nm, nm, nm}; OPAQUE(c.negm4[rt]);
#pragma unroll
        for (int kt2 = 0; kt2 < 2; ++kt2)
#pragma unroll
          for (int r = 0; r < 4; ++r) c.S[X][rt][kt2][r] = fsub_s(c.S[X][rt][kt2][r], d);
        c.fct[rt] = __builtin_amdgcn_exp2f(-d);
      }
      if constexpr (!FIRST) c.pend = true;
    }
  } else {
    constexpr int J = I - 13, rt = J / 12, k = J % 12;
    if constexpr (k < 8) {
      float e = __builtin_amdgcn_exp2f(c.S[X][rt][k >> 2][k & 3]); OPAQUE(e);
      c.S[X][rt][k >> 2][k & 3] = e;
    } else {
      constexpr int p = k - 8;
      unsigned w = cvtpk(c.S[X][rt][p >> 1][2 * (p & 1)], c.S[X][rt][p >> 1][2 * (p & 1) + 1]); OPAQUE(w);
      c.P[X][rt][p] = w;
    }
  }
}
template <int X, bool FIRST, int LO, int... Is> DI void sm_ops_(St& c, iseq<Is...>) { (sm_op<X, FIRST, LO + Is>(c), ...); }
template <int X, bool FIRST, int LO, int HI> DI void sm_ops(St& c) { if constexpr (HI > LO) sm_ops_<X, FIRST, LO>(c, make_iseq<HI - LO>{}); }

constexpr int sm_cost(int i) { return i < 12 ? 4 : (i == 12 ? 8 : (((i - 13) % 12) < 8 ? 8 : 4)); }
struct SmSched { int op[75][2]; int last; int pv_done[SM_NOPS]; };
constexpr SmSched make_sched() {
  SmSched s{};
  int at[SM_NOPS] = {};
  for (int i = 0; i < SM_NOPS; ++i) at[i] = -1;
  s.last = 0;
  for (int gi = 0; gi < 75; ++gi) {
    s.op[gi][0] = -1; s.op[gi][1] = -1;
    const bool pv = gi < 39;
    const int m = pv ? gi : gi - 39, grp = m / 3, r = m % 3;
    int cap = 8;
    if (pv) { if (grp < 9) { if (r < 2) cap = 4; } else if (grp < 12) { if (r == 0) cap = 4; } }
    else {
      if (grp < 9) { if (r == 0) cap = 4; } else { if (r < 2) cap = 4; }
      if (grp < 4 && r == 2) cap = 4;
      if (grp == 6 && r == 1) cap = 4;
      if (grp == 8 && r >= 1) cap = 0;
      if ((grp == 9 || grp == 10) && r == 2) cap = 4;
    }
    int n = 0;
    for (int pass = 0; pass < 2; ++pass)
      for (int i = 0; i < SM_NOPS && n < 2; ++i) {
        if (at[i] >= 0) continue;
        const int cst = sm_cost(i);
        if ((pass == 0) != (cst == 8) || cst > cap) continue;
        bool ok = true;
        if (i < 12) { if (i >= 3) ok = at[i - 3] >= 0 && at[i - 3] < gi; if (i >= 6 && gi < 4) ok = false; }
        else if (i == 12) { for (int k = 0; k < 12; ++k) ok = ok && at[k] >= 0 && at[k] < gi; }
        else {
          const int J = i - 13, rt = J / 12, k = J % 12;
          if (k < 8) ok = at[12] >= 0 && at[12] < gi;
          else { const int e0 = 13 + rt * 12 + 2 * (k - 8); ok = at[e0] >= 0 && at[e0] < gi && at[e0 + 1] >= 0 && at[e0 + 1] < gi; }
        }
        if (!ok) continue;
        at[i] = gi; s.op[gi][n++] = i; cap -= cst; s.last = gi;
      }
  }
  for (int i = 0; i < SM_NOPS; ++i) { s.pv_done[i] = (at[i] >= 0 && at[i] < 39) ? 1 : 0; if (at[i] < 0) s.last = 1000; }
  return s;
}
constexpr SmSched SCHED_N = make_sched();
static_assert(SCHED_N.last <= 72, "softmax schedule must finish before the last gaps of the QK phase");
template <int X, int... Is> DI void sm_rest_(St& c, iseq<Is...>) { ((SCHED_N.pv_done[Is] ? (void)0 : sm_op<X, false, Is>(c)), ...); }

template <int... Is> DI void rescale_(St& c, iseq<Is...>) { (acc_scale4<4 * Is>(c.fct[Is / 13]), ...); }
DI void rescale(St& c) {
  asm volatile("s_nop 7" ::: "memory");
  rescale_(c, make_iseq<39>{});
  asm volatile("s_nop 1" ::: "memory");
}

constexpr int EP_NOPS = 6 + 18 * 24;
constexpr int ASTASH = 156;
template <bool DIRECT, int I0> DI void ep_op(St& c) {
  if constexpr (I0 < 6) {
    constexpr int rt = I0 % 3;
    if constexpr (I0 < 3) c.inv[rt] = acc_read<AO(rt, 12)>();
    else { c.inv[rt] = __builtin_amdgcn_rcpf(c.inv[rt]); OPAQUE(c.inv[rt]); }
  } else {
    constexpr int I = I0 - 6, q = I / 24, k = I % 24, ES = q / 3, rt = q % 3;
    if constexpr (k < 8) c.et[k] = acc_read<AO(rt, 2 * ES + (k >> 2)) + (k & 3)>();
    else if constexpr (k < 16) { c.et[k - 8] = c.et[k - 8] * c.inv[rt]; OPAQUE(c.et[k - 8]); }
    else if constexpr (k < 20) { unsigned w = cvtpk(c.et[2 * (k - 16)], c.et[2 * (k - 16) + 1]); OPAQUE(w); c.ew[k - 16] = w; }
    else if constexpr (DIRECT) { if constexpr (k == 20) __builtin_amdgcn_raw_buffer_store_b128(c.ew, c.rO, c.voO, (ES * 9 + rt) * 1024, O_AUX); }
    else asm volatile("v_accvgpr_write_b32 a[%c1], %0" :: "v"(c.ew[k - 20]), "i"(ASTASH + 4 * q + (k - 20)));
  }
}
template <bool DIRECT, int LO, int... Is> DI void ep_ops_(St& c, iseq<Is...>) { (ep_op<DIRECT, LO + Is>(c), ...); }
template <bool DIRECT, int LO, int HI> DI void ep_ops(St& c) {
  constexpr int H2 = HI > EP_NOPS ? EP_NOPS : HI;
  if constexpr (H2 > LO) ep_ops_<DIRECT, LO>(c, make_iseq<H2 - LO>{});
}
template <int Q> DI void stash_store(St& c) {
  constexpr int ES = Q / 3, rt = Q % 3;
  asm volatile("buffer_store_dwordx4 a[%c3:%c4], %0, %1, %2 offen" :: "v"(c.voO), "s"(c.rO), "s"((unsigned)((ES * 9 + rt) * 1024)), "i"(ASTASH + 4 * Q), "i"(ASTASH + 4 * Q + 3) : "memory");
}

constexpr int ep_slot(int M) {
  int n = 0;
  for (int m = 0; m < M; ++m) {
    const int g = m / 3, r = m % 3;
    int cap = (r < 2) ? 1 : 2;
    if (r == 2 && g >= 6 && g < 9) cap = 1;
    if (r == 2 && g == 10) cap = 0;
    if (r == 2 && g == 16) cap = 1;
    if (g >= 17) cap = (r < 2 && g < 20) ? 1 : ((r == 2 && g == 18) ? 2 : 0);
    n += cap;
  }
  return n;
}
constexpr int EP_PER = ep_slot(72);
static_assert(EP_PER * 7 >= EP_NOPS, "epilogue ops must fit in 7 Q-projection steps");
template <int PH, int HALF, int X, int NPH, int NHALF, int NL0, int NL1, int ND, bool Z, int SM, int ES, int SQ_, int M>
DI void gap(St& c, unsigned nb) {
  constexpr bool SYNC = NL0 != -1, RTS = NL0 == -2;
  constexpr int NGF = (PH == PH_QP) ? 24 : 12;
  constexpr int g = M / 3, rt = M % 3;
  constexpr unsigned S0 = NL0 >= 0 ? (unsigned)(NL0 % 6) * SLOT : 0u, S1 = NL1 >= 0 ? (unsigned)(NL1 % 6) * SLOT : 0u;
#define ADD0(v) (RTS ? vadd_s(c.rs0, v) : vadd_c<S0>(v))
#define ADD1(v) (RTS ? vadd_s(c.rs1, v) : vadd_c<S1>(v))
  if constexpr (PH == PH_QP) {
    constexpr int ks = g / 12, t = g % 12;
    const bf16x8 f = cat4(c.flo[g & 3], c.fhi[g & 3]);
    if constexpr (Z && ks == 0) asm volatile("v_mfma_f32_16x16x32_bf16 %0, %1, %2, 0" : "=&v"(c.qa[rt][t]) : "v"(f), "v"(c.xf[ks][rt]));
    else                        asm volatile("v_mfma_f32_16x16x32_bf16 %0, %1, %2, %0" : "+v"(c.qa[rt][t]) : "v"(f), "v"(c.xf[ks][rt]));
  } else if constexpr (PH == PH_QK) {
    constexpr int kt2 = g / 6, s = g % 6;
    if constexpr (s == 0) asm volatile("v_mfma_f32_16x16x32_bf16 %0, %1, %2, %3" : "=&v"(c.S[X][rt][kt2]) : "v"(c.fk[g & 3]), "v"(c.qf[rt][s]), "v"(c.negm4[rt]));
    else                  asm volatile("v_mfma_f32_16x16x32_bf16 %0, %1, %2, %0" : "+v"(c.S[X][rt][kt2]) : "v"(c.fk[g & 3]), "v"(c.qf[rt][s]));
  } else {
    const bf16x8 pf = __builtin_bit_cast(bf16x8, c.P[X][rt]);
    if constexpr (g < 12) mfma_acc<AO(rt, g), Z>(cat4(c.flo[g & 3], c.fhi[g & 3]), pf);
    else                  mfma_acc<AO(rt, 12), Z>(c.ones, pf);
  }
  GAP();
  if constexpr (PH == PH_QK && M == 24) asm volatile("" :: "v"(c.negm4[0]), "v"(c.negm4[1]), "v"(c.negm4[2]));
  if constexpr (rt < 2) {
    if constexpr (g + 3 < NGF) read_frag<PH, HALF, g + 3, rt>(c);
    else if constexpr (g + 3 - NGF < 3) read_frag<NPH, NHALF, g + 3 - NGF, rt>(c);
  }
  if constexpr (PH == PH_QP && g >= 6 && g < 9 && rt == 2) read_x<1, g - 6>(c);
  if constexpr (NPH == PH_QP && g == NGF - 3 && rt == 2) { read_x<0, 0>(c); read_x<0, 1>(c); }
  if constexpr (NPH == PH_QP && g == NGF - 2 && rt == 2) read_x<0, 2>(c);
  if constexpr (SYNC) {
    if constexpr (PH == PH_QK ? (g == 6 && rt == 1) : (g == NGF - 8 && rt == 2)) flag_read(c);
    if constexpr (PH == PH_QK) {
      if constexpr (g == 8 && rt == 1) flag_wait(c, nb + ND);
      if constexpr (g == 8 && rt == 2) done_signal(c);
      if constexpr (g < 4 && rt == 2) c.bV[g] = ADD1(c.aV[g]);
      if constexpr (g == 9 && rt == 2) c.bK0 = ADD0(c.aK0);
      if constexpr (g == 10 && rt == 2) c.bK1 = ADD0(c.aK1);
    } else {
      if constexpr (g == NGF - 5 && rt == 2) flag_wait(c, nb + ND);
      if constexpr (g == NGF - 4 && rt == 2) done_signal(c);
      if constexpr (NPH == PH_QK) {
        if constexpr (g == 10 && rt == 2) { c.bK0 = ADD0(c.aK0); c.bK1 = ADD0(c.aK1); }
      } else if constexpr (PH == PH_QP) {
        if constexpr (g == 10 && rt == 2) { c.bX0 = ADD0(c.aX0); c.bX1 = ADD0(c.aX1); }
        if constexpr (g == 17 && rt == 2) c.bW[0] = ADD1(c.aW[0]);
        if constexpr (g == 20 && rt == 0) c.bW[1] = ADD1(c.aW[1]);
        if constexpr (g == 20 && rt == 1) c.bW[2] = ADD1(c.aW[2]);
        if constexpr (g == 22 && rt == 2) c.bW[3] = ADD1(c.aW[3]);
      } else {
        if constexpr (g == 0 && rt == 2) { c.bX0 = ADD0(c.aX0); c.bX1 = ADD0(c.aX1); }
        if constexpr (g == 1 && rt == 2) { c.bW[0] = ADD1(c.aW[0]); c.bW[1] = ADD1(c.aW[1]); }
        if constexpr (g == 2 && rt == 2) { c.bW[2] = ADD1(c.aW[2]); c.bW[3] = ADD1(c.aW[3]); }
      }
    }
  }
  if constexpr (SM == 1) {
    constexpr int gi = (PH == PH_PV) ? M : 39 + M;
    if constexpr (SCHED_N.op[gi][0] >= 0) sm_op<X ^ 1, false, SCHED_N.op[gi][0]>(c);
    if constexpr (SCHED_N.op[gi][1] >= 0) sm_op<X ^ 1, false, SCHED_N.op[gi][1]>(c);
  } else if constexpr (SM == 2) {
    if constexpr (M >= 24 && M < 30) sm_ops<X, true, M - 24, M - 23>(c);
  } else if constexpr (SM == 3) {
    if constexpr (M < 34) sm_ops<X ^ 1, true, 13 + M, 14 + M>(c);
    if constexpr (M == 33) sm_ops<X ^ 1, true, 13 + 34, 13 + 36>(c);
  }
  if constexpr (ES >= 0) {
    constexpr int e0 = ES * EP_PER + ep_slot(M), e1 = ES * EP_PER + ep_slot(M + 1);
    ep_ops<false, e0, e1>(c);
  }
  if constexpr (SQ_ >= 0 && M == (PH == PH_QP ? 71 : (PH == PH_QK ? 35 : 38))) stash_store<SQ_>(c);
  GAP();
}
template <int PH, int HALF, int X, int NPH, int NHALF, int NL0, int NL1, int ND, bool Z, int SM, int ES, int SQ_, int... Ms>
DI void phase_(St& c, unsigned nb, iseq<Ms...>) { (gap<PH, HALF, X, NPH, NHALF, NL0, NL1, ND, Z, SM, ES, SQ_, Ms>(c, nb), ...); }
template <int PH, int HALF, int X, int NPH, int NHALF, int NL0, int NL1, int ND, bool Z, int SM, int ES = -1, int SQ_ = -1>
DI void phase(St& c, unsigned nb) {
  constexpr int NM = (PH == PH_QP) ? 72 : (PH == PH_QK ? 36 : 39);
  phase_<PH, HALF, X, NPH, NHALF, NL0, NL1, ND, Z, SM, ES, SQ_>(c, nb, make_iseq<NM>{});
  if constexpr (PH == PH_PV && SM == 1) {
    if (__builtin_expect(c.pend, 0)) { rescale(c); c.pend = false; }
    GAP();
  }
}

template <int RT, int S> DI void q_convert(St& c) {
  const f32x4 a = c.qa[RT][2 * S] * QSCALE, b = c.qa[RT][2 * S + 1] * QSCALE;
  unsigned pa0 = cvtpk(a[0], a[1]), pa1 = cvtpk(a[2], a[3]);
  unsigned pb0 = cvtpk(b[0], b[1]), pb1 = cvtpk(b[2], b[3]);
  auto r0 = __builtin_amdgcn_permlane32_swap(pa0, pb0, false, false);
  auto r1 = __builtin_amdgcn_permlane32_swap(pa1, pb1, false, false);
  auto t0 = __builtin_amdgcn_permlane16_swap(r0[0], r0[1], false, false);
  auto t1 = __builtin_amdgcn_permlane16_swap(r1[0], r1[1], false, false);
  u32x4 w = {t0[0], t1[0], t0[1], t1[1]};
  c.qf[RT][S] = __builtin_bit_cast(bf16x8, w);
}
template <int... Is> DI void q_convert_all(St& c, iseq<Is...>) { (q_convert<Is % 3, Is / 3>(c), ...); }

__host__ __device__ constexpr unsigned slot_of(int n) { return (unsigned)(n % 6) * SLOT; }
__host__ __device__ constexpr int kload(int j) { return j == 0 ? 20 : 2 * j + 19; }
__host__ __device__ constexpr int vload(int j) { return j == 7 ? 35 : 2 * j + 22; }

struct Ld {
  __amdgpu_buffer_rsrc_t rX, rK, rV, rW;
  unsigned vK[6], vV[3], vW[12], vX[4], vL[3];
  unsigned ldsb, sW[2], sX[2], sK[2];
  u32x4 buf[2][24];
  __amdgpu_buffer_rsrc_t rWO, rWF;
  unsigned sWO, sWF, vWO, vWA, vWR, vWF;
  unsigned base_step, base_load;
};
#define DMA16I(rsrc, ldsoff, voff, soff, imm, aux) \
  __builtin_amdgcn_raw_ptr_buffer_load_lds((rsrc), (LAS void*)(uintptr_t)(ldsoff), 16, (voff), (soff), (imm), (aux))
constexpr int c_hi(int T) { return T < 0 ? (T == -1 ? 0 : -1) : (T < 10 ? 2 * T + 2 : (T == 10 ? 21 : (T < 18 ? 2 * T + 1 : (T == 18 ? 36 : 38)))); }
#define NFIFO 8
static_assert(NFIFO <= 8, "FIFO entry 2 is reused by the wo re-layout from step 13 on");
constexpr int xw_instr(int N) { return ((N % 36) & 1) == 0 ? 18 : 24; }
struct LSeq { int id[96]; int cnt[96]; int step[96]; int n; };
constexpr int kv_chunks_at(int T, int k) {
  if (T == 17) return k < 2 ? k : -1;
  if (T == 18) return k < 2 ? 2 + k : -1;
  if (T < 8) return k < 1 ? 4 + T : -1;
  return -1;
}
constexpr LSeq make_lseq() {
  LSeq q{}; q.n = 0;
  for (int T = 0; T < 19; ++T) {
    const int lo = c_hi(T - 2) + 6, hi = c_hi(T - 1) + 6;
    for (int n = lo; n < hi; ++n) {
      if ((n % 36) < 20) { q.id[q.n] = n; q.cnt[q.n] = xw_instr(n); q.step[q.n] = T; ++q.n; }
      else if (n - 20 >= NFIFO) { q.id[q.n] = n; q.cnt[q.n] = 24; q.step[q.n] = T; ++q.n; }
      else { const int i = n - 20; if (i + 4 < NFIFO) for (int cch = 0; cch < 3; ++cch) { q.id[q.n] = 100 + 3 * (i + 4) + cch; q.cnt[q.n] = 8; q.step[q.n] = T; ++q.n; } }
    }
    if (T == 13) { q.id[q.n] = 300; q.cnt[q.n] = 20; q.step[q.n] = T; ++q.n; }
    if (T == 15) { q.id[q.n] = 301; q.cnt[q.n] = 20; q.step[q.n] = T; ++q.n; }
    for (int k = 0; k < 2; ++k) {
      const int ch = kv_chunks_at(T, k);
      if (ch >= 0) { q.id[q.n] = 100 + (T >= 17 ? 48 : 0) + ch; q.cnt[q.n] = 8; q.step[q.n] = T; ++q.n; }
    }
  }
  return q;
}
constexpr LSeq LSEQ = make_lseq();
constexpr int lseq_pos(int id) { for (int k = 0; k < LSEQ.n; ++k) if (LSEQ.id[k] == id) return k; return -1; }
constexpr int younger(int id, int cur) {
  int p = lseq_pos(id);
  int r = 0;
  if (p >= 0 && p < cur) { for (int k = p + 1; k < cur; ++k) r += LSEQ.cnt[k]; return r; }
  const int pid = id < 100 ? id + 36 : id + 48;
  p = lseq_pos(pid);
  for (int k = p + 1; k < LSEQ.n; ++k) r += LSEQ.cnt[k];
  for (int k = 0; k < cur; ++k) r += LSEQ.cnt[k];
  return r;
}
template <int N> DI void vm_wait() { asm volatile("s_waitcnt vmcnt(%0)" :: "i"(N > 63 ? 63 : N) : "memory"); }

template <int N, int P> DI void xw_piece(Ld& ld) {
  constexpr int n = N % 36, nx = N / 36;
  const unsigned l0 = ld.ldsb + slot_of(n) + (P >> 2) * 4096;
  constexpr int imm = (P & 3) * 1024;
  if constexpr ((n & 1) == 0) DMA16I(ld.rX, l0, ld.vX[P & 3], ld.sX[nx] + (n >> 1) * 128 + (P >> 2) * (32 * DM * 2), imm, 0);
  else                        DMA16I(ld.rW, l0, ld.vW[P % 12], ld.sW[nx] + (unsigned)((n >> 1) * 64 + (P / 12) * 32) * (HD * 2), imm, 0);
}
template <int N, int... Ps> DI void xw_load_(Ld& ld, iseq<Ps...>) { (xw_piece<N, Ps>(ld), ...); }
template <int N> DI void xw_load(Ld& ld) { xw_load_<N>(ld, make_iseq<xw_instr(N)>{}); }

constexpr bool kv_isK(int i) { return i == 0 || (i < 15 && (i & 1)); }
constexpr int kv_j(int i) { return kv_isK(i) ? (i + 1) >> 1 : (i == 15 ? 7 : (i - 2) >> 1); }
template <int I, int P> DI void kv_issue_piece(Ld& ld) {
  constexpr int i = I % 16, nx = I / 16, e = i & 3, R = 96 * e + 4 * P;
  const unsigned so = ld.sK[nx] + kv_j(i) * SLOT + (P >> 2) * 4096;
  const unsigned vo = kv_isK(i) ? ld.vK[P % 6] : ld.vV[P % 3];
  const __amdgpu_buffer_rsrc_t rs = kv_isK(i) ? ld.rK : ld.rV;
  if constexpr (e < 2) asm volatile("buffer_load_dwordx4 a[%c3:%c4], %0, %1, %2 offen offset:%c5 nt" :: "v"(vo), "s"(rs), "s"(so), "i"(R), "i"(R + 3), "i"((P & 3) * 1024) : "memory");
  else                 asm volatile("buffer_load_dwordx4 %0, %1, %2, %3 offen offset:%c4 nt" : "=v"(ld.buf[e - 2][P]) : "v"(vo), "s"(rs), "s"(so), "i"((P & 3) * 1024) : "memory");
}
template <int I, int P0, int... Ps> DI void kv_issue_(Ld& ld, iseq<Ps...>) { (kv_issue_piece<I, P0 + Ps>(ld), ...); }
template <int I, int CH> DI void kv_chunk(Ld& ld) { kv_issue_<I, 8 * CH>(ld, make_iseq<8>{}); }
template <int I, int P> DI void kv_write_piece(Ld& ld) {
  constexpr int e = I & 3, R = 96 * e + 4 * P, sl = (20 + I) % 6;
  constexpr int off = (sl & 1) * SLOT + P * 1024;
  if constexpr (e < 2) asm volatile("ds_write_b128 %0, a[%c1:%c2] offset:%c3" :: "v"(ld.vL[sl >> 1]), "i"(R), "i"(R + 3), "i"(off) : "memory");
  else                 asm volatile("ds_write_b128 %0, %1 offset:%c2" :: "v"(ld.vL[sl >> 1]), "v"(ld.buf[e - 2][P]), "i"(off) : "memory");
}
template <int I, int... Ps> DI void kv_write_(Ld& ld, iseq<Ps...>) { (kv_write_piece<I, Ps>(ld), ...); }

DI void publish(Ld& ld, unsigned v) {
  asm volatile("ds_write_b32 %0, %1" :: "v"(ld.ldsb + FLAG_OFF), "v"(v) : "memory");
}
DI void wait_done(Ld& ld, unsigned need) {
  while (true) {
    u32x4 d;
    asm volatile("ds_read_b128 %0, %1\n\ts_waitcnt lgkmcnt(0)" : "=v"(d) : "v"(ld.ldsb + FLAG_OFF + 16) : "memory");
    const unsigned m = __builtin_amdgcn_readfirstlane(min(min(d[0], d[1]), d[2]));
    if (m >= need) break;
    __builtin_amdgcn_s_sleep(1);
  }
}
constexpr int lseq_before(int T, int n) {
  int k = 0;
  while (k < LSEQ.n && LSEQ.step[k] < T) ++k;
  const int lo = c_hi(T - 2) + 6, hi = c_hi(T - 1) + 6;
  for (int m = lo; m < hi && m < n; ++m) { if ((m % 36) < 20 || m - 20 >= NFIFO) k += 1; else if (m - 20 + 4 < NFIFO) k += 3; }
  return k;
}
template <int I, int P> DI void kv_dma_piece(Ld& ld) {
  const unsigned l0 = ld.ldsb + slot_of(20 + I) + (P >> 2) * 4096;
  constexpr int imm = (P & 3) * 1024;
  if constexpr (kv_isK(I)) DMA16I(ld.rK, l0, ld.vK[P % 6], ld.sK[0] + kv_j(I) * SLOT + (P >> 2) * 4096, imm, KV_AUX);
  else                     DMA16I(ld.rV, l0, ld.vV[P % 3], ld.sK[0] + kv_j(I) * SLOT + (P >> 2) * 4096, imm, KV_AUX);
}
template <int I, int... Ps> DI void kv_dma_(Ld& ld, iseq<Ps...>) { (kv_dma_piece<I, Ps>(ld), ...); }
template <int T, int N> DI void loader_load(Ld& ld) {
  if constexpr ((N % 36) < 20) xw_load<N>(ld);
  else if constexpr (N - 20 >= NFIFO) kv_dma_<N - 20>(ld, make_iseq<24>{});
  else {
    constexpr int i = N - 20;
    vm_wait<younger(100 + 3 * i + 2, lseq_before(T, N))>();
    kv_write_<i>(ld, make_iseq<24>{});
    asm volatile("s_waitcnt lgkmcnt(0)" ::: "memory");
    publish(ld, ld.base_load + N + 1);
    if constexpr (i + 4 < NFIFO) { kv_chunk<i + 4, 0>(ld); kv_chunk<i + 4, 1>(ld); kv_chunk<i + 4, 2>(ld); }
  }
}
template <int I> DI void wo_load(Ld& ld) {
  asm volatile("buffer_load_dwordx4 %0, %1, %2, %3 offen offset:%c4" : "=v"(ld.buf[0][I]) : "v"(ld.vWO), "s"(ld.rWO), "s"(ld.sWO + (I & 3) * (8 * DM * 2)), "i"((I >> 2) * 128) : "memory");
}
template <int... Is> DI void wo_loads(Ld& ld, iseq<Is...>) { (wo_load<Is>(ld), ...); }
template <int CH> DI void wo_chunk(Ld& ld) {
  u32x2 a0, a1, a2, a3, b0, b1, b2, b3;
  asm volatile("ds_write_b128 %8, %10\n\tds_write_b128 %8, %11 offset:1280\n\tds_write_b128 %8, %12 offset:2560\n\tds_write_b128 %8, %13 offset:3840\n\t"
               "ds_read_b64_tr_b16 %0, %9\n\tds_read_b64_tr_b16 %4, %9 offset:2560\n\t"
               "ds_read_b64_tr_b16 %1, %9 offset:32\n\tds_read_b64_tr_b16 %5, %9 offset:2592\n\t"
               "ds_read_b64_tr_b16 %2, %9 offset:64\n\tds_read_b64_tr_b16 %6, %9 offset:2624\n\t"
               "ds_read_b64_tr_b16 %3, %9 offset:96\n\tds_read_b64_tr_b16 %7, %9 offset:2656\n\t"
               "s_waitcnt lgkmcnt(0)"
               : "=&v"(a0), "=&v"(a1), "=&v"(a2), "=&v"(a3), "=&v"(b0), "=&v"(b1), "=&v"(b2), "=&v"(b3)
               : "v"(ld.vWA), "v"(ld.vWR), "v"(ld.buf[0][CH * 4]), "v"(ld.buf[0][CH * 4 + 1]), "v"(ld.buf[0][CH * 4 + 2]), "v"(ld.buf[0][CH * 4 + 3]) : "memory");
#define WO_ST(t, lo, hi) { constexpr int nt = CH * 4 + t; u32x4 w = {lo[0], lo[1], hi[0], hi[1]}; \
    __builtin_amdgcn_raw_buffer_store_b128(w, ld.rWF, ld.vWF, ld.sWF + ((nt / 5) * 192 * 5 + nt % 5) * 1024, 0); }
  WO_ST(0, a0, b0) WO_ST(1, a1, b1) WO_ST(2, a2, b2) WO_ST(3, a3, b3)
#undef WO_ST
}
template <int T, int LO, int... Is> DI void loader_loads_(Ld& ld, iseq<Is...>) { (loader_load<T, LO + Is>(ld), ...); }
template <int T> DI void loader_step(Ld& ld) {
  constexpr int lo = c_hi(T - 2) + 6, hi = c_hi(T - 1) + 6, need = c_hi(T + 1);
  asm volatile("" : "+s"(ld.ldsb), "+s"(ld.sW[0]), "+s"(ld.sX[0]), "+s"(ld.sK[0]));
  wait_done(ld, ld.base_step + T);
  if constexpr (T == 8) {
    vm_wait<younger(19, lseq_before(T, 0))>();
    publish(ld, ld.base_load + 20);
  }
  loader_loads_<T, lo>(ld, make_iseq<hi - lo>{});
  if constexpr (T != 8 && (need <= 20 || need - 1 - 20 >= NFIFO)) {
    vm_wait<younger(need - 1, lseq_before(T, 1000))>();
    publish(ld, ld.base_load + need);
  }
  if constexpr (T == 13) wo_loads(ld, make_iseq<20>{});
  if constexpr (T == 15) {
    vm_wait<younger(300, lseq_before(T, 1000))>();
    wo_chunk<0>(ld); wo_chunk<1>(ld); wo_chunk<2>(ld); wo_chunk<3>(ld); wo_chunk<4>(ld);
    asm volatile("" ::: "memory");
  }
  if constexpr (kv_chunks_at(T, 0) >= 0) { constexpr int ch = kv_chunks_at(T, 0); kv_chunk<(T >= 17 ? 16 : 0) + ch / 3, ch % 3>(ld); }
  if constexpr (kv_chunks_at(T, 1) >= 0) { constexpr int ch = kv_chunks_at(T, 1); kv_chunk<(T >= 17 ? 16 : 0) + ch / 3, ch % 3>(ld); }
}
template <int... Ts> DI void loader_unit(Ld& ld, iseq<Ts...>) { (loader_step<Ts>(ld), ...); }

__global__ __launch_bounds__(256, 1)
void attn_fused_kernel(const uint16_t* __restrict__ xA, const uint16_t* __restrict__ kA, const uint16_t* __restrict__ vA,
                       const uint16_t* __restrict__ wqA, const uint16_t* __restrict__ woA,
                       const uint16_t* __restrict__ xB, const uint16_t* __restrict__ kB, const uint16_t* __restrict__ vB,
                       const uint16_t* __restrict__ wqB, const uint16_t* __restrict__ woB,
                       uint16_t* __restrict__ Ows, uint16_t* __restrict__ WOws) {
  extern __shared__ __attribute__((aligned(16))) char smem[];
  const unsigned ldsb = (unsigned)(uintptr_t)(LAS char*)smem;
  const int tid = threadIdx.x;
  const int wave = __builtin_amdgcn_readfirstlane(tid >> 6);
  const int l = tid & 63;
  const int wg = blockIdx.x;
  const int xcd = wg & 7, wi = wg >> 3;
  const int blk = xcd >> 2;
  const int head = (xcd & 3) * 8 + (wi & 7);
  const int bsub = wi >> 3;

  if (tid < 16) *(volatile LAS unsigned*)(uintptr_t)(ldsb + FLAG_OFF + tid * 4) = 0u;
  asm volatile("s_waitcnt lgkmcnt(0)\n\ts_barrier" ::: "memory");

  if (wave == 3) {
    const uint16_t* xp = blk ? xB : xA; const uint16_t* kp = blk ? kB : kA;
    const uint16_t* vp = blk ? vB : vA; const uint16_t* wqp = blk ? wqB : wqA;
    Ld ld;
    asm volatile("" ::: "a191");
    ld.ldsb = ldsb;
    const unsigned l3 = l >> 3, l7 = l & 7;
#pragma unroll
    for (int p = 0; p < 12; ++p) {
      const unsigned line = 8 * p + l3, row = line / 3, seg = line % 3;
      const unsigned f = (((row >> 1) & 1) | ((row >> 2) & 2)) << 1;
      ld.vW[p] = row * (HD * 2) + seg * 128 + ((l7 ^ f) << 4) - (p & 3) * 1024;
      if (p < 6) ld.vK[p] = l3 * 128 + ((l7 ^ ((row >> 1) & 7)) << 4);
      if (p < 3) ld.vV[p] = l3 * 128 + ((l7 ^ (((row >> 1) & 3) << 1)) << 4);
      if (p < 4) ld.vX[p] = line * (DM * 2) + ((l7 ^ ((line >> 1) & 7)) << 4) - p * 1024;
      if (p < 3) ld.vL[p] = ldsb + p * 2 * SLOT + l * 16;
    }
    ld.sW[0] = ld.sW[1] = head * (DH * 2);
    ld.rX = __builtin_amdgcn_make_buffer_rsrc((void*)xp, 0, NB * SQ * DM * 2, 0x00020000);
    ld.rK = __builtin_amdgcn_make_buffer_rsrc((void*)kp, 0, NB * NH * SK * DH * 2, 0x00020000);
    ld.rV = __builtin_amdgcn_make_buffer_rsrc((void*)vp, 0, NB * NH * SK * DH * 2, 0x00020000);
    ld.rW = __builtin_amdgcn_make_buffer_rsrc((void*)wqp, 0, DM * HD * 2, 0x00020000);
    ld.sX[1] = (unsigned)(bsub * SQ) * (DM * 2);
    ld.sK[1] = (unsigned)((bsub * NH + head) * SK) * (DH * 2);
    const int rb = wg >> 1, nh = wg & 1, b2 = rb >> 6, ks0 = (rb & 63) * 3;
    ld.rWO = __builtin_amdgcn_make_buffer_rsrc((void*)(b2 ? woB : woA), 0, HD * DM * 2, 0x00020000);
    ld.rWF = __builtin_amdgcn_make_buffer_rsrc((void*)WOws, 0, 2 * HD * DM * 2, 0x00020000);
    ld.vWO = l3 * (DM * 2) + l7 * 16;
    ld.vWA = ldsb + SCR_OFF + l3 * 160 + l7 * 16;
    ld.vWR = ldsb + SCR_OFF + (4 * (l >> 4) + ((l >> 2) & 3)) * 160 + (l & 3) * 8;
    ld.vWF = l * 16;
    xw_load<36>(ld);
    xw_load<37>(ld); xw_load<38>(ld); kv_chunk<16, 0>(ld); kv_chunk<16, 1>(ld);
    xw_load<39>(ld); xw_load<40>(ld);
    vm_wait<younger(37, lseq_before(18, 1000))>();
    publish(ld, 2);
    kv_chunk<16, 2>(ld); kv_chunk<17, 0>(ld);
    for (int u = 0; u < 4; ++u) {
      ld.base_step = 19 * u; ld.base_load = 36 * u;
      ld.sX[0] = ld.sX[1]; ld.sK[0] = ld.sK[1];
      ld.sX[1] = (u < 3) ? ld.sX[0] + 4 * SQ * (DM * 2) : 0x40000000u;
      ld.sK[1] = (u < 3) ? ld.sK[0] + 4 * NH * SK * (DH * 2) : 0x40000000u;
      ld.sW[1] = (u < 3) ? ld.sW[0] : 0x40000000u;
      ld.sWO = (u < 3) ? (unsigned)(32 * (ks0 + u)) * (DM * 2) + nh * 640 : 0x40000000u;
      ld.sWF = (u < 3) ? (unsigned)(((b2 * 8 + nh * 4) * 192 + ks0 + u) * 5) * 1024 : 0x40000000u;
      asm volatile("" ::: ACLOB, ACLOB2);
      loader_unit(ld, make_iseq<19>{});
    }
    asm volatile("s_waitcnt vmcnt(0)" ::: "memory");
    return;
  }

  const int cc = l & 15, g = l >> 4, q4 = (l >> 2) & 3, p4 = l & 3;
  St c;
  {
    const unsigned swz8 = (cc >> 1) & 7;
    c.aX0 = ldsb + (48 * wave + cc) * 128 + ((g ^ swz8) << 4);
    c.aX1 = ldsb + (48 * wave + cc) * 128 + (((4 + g) ^ swz8) << 4);
    c.aK0 = ldsb + cc * 384 + ((g ^ swz8) << 4);
    c.aK1 = ldsb + cc * 384 + (((4 + g) ^ swz8) << 4);
    const unsigned fp = ((q4 >> 1) & 1) | ((g & 1) << 1);
#pragma unroll
    for (int i = 0; i < 4; ++i) {
      const unsigned inrow = ((i ^ fp) << 5) + ((p4 >> 1) << 4) + ((p4 & 1) << 3);
      c.aW[i] = ldsb + (8 * g + q4) * 384 + inrow;
      c.aV[i] = ldsb + (4 * g + q4) * 384 + inrow;
    }
    u32x4 o1 = {0x3F803F80u, 0x3F803F80u, 0x3F803F80u, 0x3F803F80u};
    c.ones = __builtin_bit_cast(bf16x8, o1); OPAQUE(c.ones);
    c.pend = false;
    c.aflag = ldsb + FLAG_OFF; c.adone = ldsb + FLAG_OFF + 16 + wave * 4; c.step = 0;
    OPAQUE(c.aflag); OPAQUE(c.adone); OPAQUE(c.step);
    c.voO = l * 16;
    c.rO = __builtin_amdgcn_make_buffer_rsrc((void*)Ows, 0, 0, 0x00020000);
    asm volatile("" ::: ACLOB, ACLOB2);
  }

  flag_read(c); flag_wait(c, 2);
  c.bX0 = vadd_c<slot_of(0)>(c.aX0); c.bX1 = vadd_c<slot_of(0)>(c.aX1);
  c.bW[0] = vadd_c<slot_of(1)>(c.aW[0]); c.bW[1] = vadd_c<slot_of(1)>(c.aW[1]); c.bW[2] = vadd_c<slot_of(1)>(c.aW[2]); c.bW[3] = vadd_c<slot_of(1)>(c.aW[3]);
  read_frag<PH_QP, 0, 0, 0>(c); read_frag<PH_QP, 0, 0, 1>(c);
  read_x<0, 0>(c); read_x<0, 1>(c); read_x<0, 2>(c);
  read_frag<PH_QP, 0, 1, 0>(c); read_frag<PH_QP, 0, 1, 1>(c);
  read_frag<PH_QP, 0, 2, 0>(c); read_frag<PH_QP, 0, 2, 1>(c);

  for (int u = 0; u < 4; ++u) {
    const int batch = 4 * u + bsub;
    const unsigned nb = 36 * u;
    asm volatile("" ::: ACLOB, ACLOB2);
    GAP();
    STAMP(0);
    phase<PH_QP, 0, 0, PH_QP, 0,  2,  3,  4, true,  0, 0, 0>(c, nb);
    phase<PH_QP, 0, 0, PH_QP, 0,  4,  5,  6, false, 0, 1, 1>(c, nb);
    phase<PH_QP, 0, 0, PH_QP, 0,  6,  7,  8, false, 0, 2, 2>(c, nb);
    phase<PH_QP, 0, 0, PH_QP, 0,  8,  9, 10, false, 0, 3, 3>(c, nb);
    phase<PH_QP, 0, 0, PH_QP, 0, 10, 11, 12, false, 0, 4, 4>(c, nb);
    phase<PH_QP, 0, 0, PH_QP, 0, 12, 13, 14, false, 0, 5, 5>(c, nb);
    STAMP(1);
    phase<PH_QP, 0, 0, PH_QP, 0, 14, 15, 16, false, 0, 6, 6>(c, nb);
    phase<PH_QP, 0, 0, PH_QP, 0, 16, 17, 18, false, 0, -1, 7>(c, nb);
    phase<PH_QP, 0, 0, PH_QP, 0, 18, 19, 20, false, 0, -1, 8>(c, nb);
    STAMP(2);
    phase<PH_QP, 0, 0, PH_QK, 0, kload(0), -1, 21, false, 0, -1, 9>(c, nb);
    STAMP(10);
    asm volatile("s_nop 7" ::: "memory");
    GAP();
    q_convert_all(c, make_iseq<18>{});
#pragma unroll
    for (int rt = 0; rt < 3; ++rt) { c.negm4[rt] = f32x4{0.f, 0.f, 0.f, 0.f}; OPAQUE(c.negm4[rt]); }
    GAP();
    asm volatile("s_nop 1" ::: "memory");
    GAP();
    STAMP(11);
    phase<PH_QK, 0, 0, PH_QK, 1, -1, -1, 0, false, 2, -1, 10>(c, nb);
    asm volatile("s_nop 7" ::: "memory");
    GAP();
    sm_ops<0, true, 6, 13>(c);
    GAP();
    asm volatile("s_nop 1" ::: "memory");
    GAP();
    phase<PH_QK, 1, 1, PH_PV, 0, kload(1), vload(0), 23, false, 3, -1, 11>(c, nb);
    phase<PH_PV, 0, 0, PH_QK, 0, -1, -1, 0, true, 1, -1, 12>(c, nb);
    phase<PH_QK, 0, 0, PH_PV, 1, -1, -1, 0, false, 1, -1, 13>(c, nb);
    phase<PH_PV, 1, 1, PH_QK, 1, -1, -1, 0, false, 1, -1, 14>(c, nb);
    phase<PH_QK, 1, 1, PH_PV, 0, kload(2), vload(1), 25, false, 1, -1, 15>(c, nb);
    c.rs0 = slot_of(25);
    for (unsigned nd = nb + 27; nd != nb + 39; nd += 2) {
      const bool lastj = nd == nb + 37;
      c.rs1 = lastj ? c.rs0 : ((c.rs0 == 5 * SLOT) ? 0u : c.rs0 + SLOT);
      phase<PH_PV, 0, 0, PH_QK, 0, -1, -1, 0, false, 1>(c, nb);
      phase<PH_QK, 0, 0, PH_PV, 1, -1, -1, 0, false, 1>(c, nb);
      phase<PH_PV, 1, 1, PH_QK, 1, -1, -1, 0, false, 1>(c, nb);
      phase<PH_QK, 1, 1, PH_PV, 0, -2, -2, 0, false, 1>(c, lastj ? nb + 36 : nd);
      c.rs0 = (c.rs0 == 5 * SLOT) ? SLOT : c.rs0 + 2 * SLOT;
    }
    phase<PH_PV, 0, 0, PH_PV, 1, -1, -1, 0, false, 1, -1, 16>(c, nb);
    sm_rest_<1>(c, make_iseq<SM_NOPS>{});
    GAP();
    asm volatile("s_nop 1" ::: "memory");
    GAP();
    phase<PH_PV, 1, 1, PH_QP, 0, 0, 1, 38, false, 0, -1, 17>(c, nb);
    STAMP(39);
    c.rO = __builtin_amdgcn_make_buffer_rsrc((void*)(Ows + ((size_t)((blk * NB + batch) * 192 + head * 6) * 9 + wave * 3) * 512), 0, 48 * 1024, 0x00020000);
    asm volatile("s_nop 7" ::: "memory");
    STAMP(40);
  }
  GAP();
  ep_ops<true, 0, EP_NOPS>(c);
}

constexpr unsigned LDS2_BYTES = 45 * 3 * 1024;
struct Op {
  u32x4 of[4][9], wf[4][5];
  __amdgpu_buffer_rsrc_t rO, rW;
  unsigned vo[9];
};
template <int ST, int I> DI void op_load(Op& o, unsigned koff) {
  if constexpr (I < 5) o.wf[ST][I] = __builtin_amdgcn_raw_buffer_load_b128(o.rW, o.vo[I], koff * 5, 0);
  else                 o.of[ST][I - 5] = __builtin_amdgcn_raw_buffer_load_b128(o.rO, o.vo[I - 5], koff * 9, 0);
}
template <int ST, bool Z, int M> DI void op_gap(Op& o, unsigned koff_next) {
  constexpr int rt = M / 5, nt = M % 5, R = 4 * M;
  if constexpr (Z) asm volatile("v_mfma_f32_16x16x32_bf16 a[%c2:%c3], %0, %1, 0" :: "v"(o.wf[ST][nt]), "v"(o.of[ST][rt]), "i"(R), "i"(R + 3));
  else             asm volatile("v_mfma_f32_16x16x32_bf16 a[%c2:%c3], %0, %1, a[%c2:%c3]" :: "v"(o.wf[ST][nt]), "v"(o.of[ST][rt]), "i"(R), "i"(R + 3));
  GAP();
  if constexpr (M % 3 == 1 && M / 3 < 14) op_load<(ST + 3) % 4, M / 3>(o, koff_next);
  GAP();
}
template <int ST, bool Z, int... Ms> DI void op_step_(Op& o, unsigned koff_next, iseq<Ms...>) { (op_gap<ST, Z, Ms>(o, koff_next), ...); }
template <int ST, bool Z> DI void op_step(Op& o, unsigned koff_next) { op_step_<ST, Z>(o, koff_next, make_iseq<45>{}); }
template <int ST, int... Is> DI void op_load_all(Op& o, unsigned koff, iseq<Is...>) { (op_load<ST, Is>(o, koff), ...); }
template <int I> DI f32x4 acc_read4() { f32x4 v; v[0] = acc_read<4 * I>(); v[1] = acc_read<4 * I + 1>(); v[2] = acc_read<4 * I + 2>(); v[3] = acc_read<4 * I + 3>(); return v; }
template <int W, int I> DI void op_red_write(unsigned rb) {
  constexpr int own = I & 3;
  if constexpr (own != W) { constexpr int slot = (W < own) ? W : W - 1; *(LAS f32x4*)(uintptr_t)(rb + (I * 3 + slot) * 1024) = acc_read4<I>(); }
}
template <int W, int I> DI void op_red_final(unsigned rb, uint16_t* outp) {
  if constexpr ((I & 3) == W) {
    f32x4 v = acc_read4<I>();
    v += *(const LAS f32x4*)(uintptr_t)(rb + (I * 3 + 0) * 1024);
    v += *(const LAS f32x4*)(uintptr_t)(rb + (I * 3 + 1) * 1024);
    v += *(const LAS f32x4*)(uintptr_t)(rb + (I * 3 + 2) * 1024);
    constexpr int rt = I / 5, nt = I % 5;
    u32x2 w = {cvtpk(v[0], v[1]), cvtpk(v[2], v[3])};
    *(u32x2*)(outp + (size_t)(rt * 16) * (2 * DM) + nt * 16) = w;
  }
}
template <int W, int... Is> DI void op_finish(unsigned rb, uint16_t* outp, iseq<Is...>) {
  (op_red_write<W, Is>(rb), ...);
  asm volatile("s_waitcnt lgkmcnt(0)\n\ts_barrier" ::: "memory");
  (op_red_final<W, Is>(rb, outp), ...);
}

__global__ __launch_bounds__(256, 1)
void oproj_kernel(const uint16_t* __restrict__ Ows, const uint16_t* __restrict__ WOws, uint16_t* __restrict__ out) {
  extern __shared__ __attribute__((aligned(16))) char smem[];
  const int tid = threadIdx.x;
  const int wave = __builtin_amdgcn_readfirstlane(tid >> 6);
  const int l = tid & 63, c = l & 15, g = l >> 4;
  const int wg = blockIdx.x, xcd = wg & 7, wi = wg >> 3;
  const int blk = xcd >> 2, batch = (xcd & 3) * 4 + (wi >> 3), ng = wi & 7;
  Op o;
  o.rO = __builtin_amdgcn_make_buffer_rsrc((void*)(Ows + ((size_t)((blk * NB + batch) * 192 + wave * 48) * 9) * 512), 0, 48 * 9 * 1024, 0x00020000);
  o.rW = __builtin_amdgcn_make_buffer_rsrc((void*)(WOws + ((size_t)((blk * 8 + ng) * 192 + wave * 48) * 5) * 512), 0, 48 * 5 * 1024, 0x00020000);
#pragma unroll
  for (int i = 0; i < 9; ++i) { o.vo[i] = l * 16 + i * 1024; OPAQUE(o.vo[i]); }
  asm volatile("" ::: ACLOB, "a156","a157","a158","a159","a160","a161","a162","a163","a164","a165","a166","a167","a168","a169","a170","a171","a172","a173","a174","a175","a176","a177","a178","a179");
  op_load_all<0>(o, 0, make_iseq<14>{});
  op_load_all<1>(o, 1024, make_iseq<14>{});
  op_load_all<2>(o, 2048, make_iseq<14>{});
  GAP();
  op_step<0, true>(o, 3 * 1024);
  op_step<1, false>(o, 4 * 1024);
  op_step<2, false>(o, 5 * 1024);
  op_step<3, false>(o, 6 * 1024);
  for (int ks = 4; ks < 48; ks += 4) {
    const unsigned k3 = (ks + 3) * 1024, k4 = (ks + 4 < 48 ? ks + 4 : 47) * 1024, k5 = (ks + 5 < 48 ? ks + 5 : 47) * 1024, k6 = (ks + 6 < 48 ? ks + 6 : 47) * 1024;
    op_step<0, false>(o, k3);
    op_step<1, false>(o, k4);
    op_step<2, false>(o, k5);
    op_step<3, false>(o, k6);
  }
  asm volatile("s_nop 7" ::: "memory");
  GAP();
  const unsigned rb = (unsigned)(uintptr_t)(LAS char*)smem + l * 16;
  uint16_t* outp = out + (size_t)(batch * SQ + c) * (2 * DM) + blk * DM + ng * 80 + 4 * g;
  if (wave == 0)      op_finish<0>(rb, outp, make_iseq<45>{});
  else if (wave == 1) op_finish<1>(rb, outp, make_iseq<45>{});
  else if (wave == 2) op_finish<2>(rb, outp, make_iseq<45>{});
  else                op_finish<3>(rb, outp, make_iseq<45>{});
}

extern "C" void kernel_launch(void* const* d_in, const int* in_sizes, int n_in,
                              void* d_out, int out_size, void* d_ws, size_t ws_size,
                              hipStream_t stream) {
  (void)in_sizes; (void)n_in; (void)out_size; (void)ws_size;
  const uint16_t* xA = (const uint16_t*)d_in[0]; const uint16_t* kA = (const uint16_t*)d_in[1];
  const uint16_t* vA = (const uint16_t*)d_in[2]; const uint16_t* wqA = (const uint16_t*)d_in[3];
  const uint16_t* woA = (const uint16_t*)d_in[4];
  const uint16_t* xB = (const uint16_t*)d_in[5]; const uint16_t* kB = (const uint16_t*)d_in[6];
  const uint16_t* vB = (const uint16_t*)d_in[7]; const uint16_t* wqB = (const uint16_t*)d_in[8];
  const uint16_t* woB = (const uint16_t*)d_in[9];
  uint16_t* Ows = (uint16_t*)d_ws;
  uint16_t* WOws = (uint16_t*)((char*)d_ws + WS_WO_OFF);
  (void)hipFuncSetAttribute((const void*)attn_fused_kernel, hipFuncAttributeMaxDynamicSharedMemorySize, (int)LDS1_BYTES);
  (void)hipFuncSetAttribute((const void*)oproj_kernel, hipFuncAttributeMaxDynamicSharedMemorySize, (int)LDS2_BYTES);
  hipLaunchKernelGGL(attn_fused_kernel, dim3(256), dim3(256), LDS1_BYTES, stream,
                     xA, kA, vA, wqA, woA, xB, kB, vB, wqB, woB, Ows, WOws);
  hipLaunchKernelGGL(oproj_kernel, dim3(256), dim3(256), LDS2_BYTES, stream, Ows, WOws, (uint16_t*)d_out);
}
```

```cpp
#include <hip/hip_runtime.h>
#include <hip/hip_bf16.h>
#include <stdint.h>
#include <utility>

typedef __bf16   bf16x8 __attribute__((ext_vector_type(8)));
typedef __bf16   bf16x4 __attribute__((ext_vector_type(4)));
typedef __bf16   bf16x2 __attribute__((ext_vector_type(2)));
typedef float    f32x4  __attribute__((ext_vector_type(4)));
typedef float    f32x2  __attribute__((ext_vector_type(2)));
typedef unsigned u32x4  __attribute__((ext_vector_type(4)));
typedef unsigned u32x2  __attribute__((ext_vector_type(2)));
#define LAS __attribute__((address_space(3)))
#define DI __device__ __forceinline__
template <int... Is> using iseq = std::integer_sequence<int, Is...>;
template <int N> using make_iseq = std::make_integer_sequence<int, N>;

constexpr int NB = 16, SQ = 144, DM = 640, NH = 32, SK = 512, DH = 192;
constexpr int HD = NH * DH;
constexpr unsigned SLOT = 24576;
constexpr unsigned SCR_OFF = 6 * SLOT;
constexpr unsigned FLAG_OFF = SCR_OFF + 5120;
constexpr unsigned LDS1_BYTES = FLAG_OFF + 64;
constexpr float QSCALE = 0.07216878364870322f * 1.4426950408889634f;
constexpr float THR = 8.0f;
constexpr float THR_LO = 32.0f;

constexpr size_t WS_O_BYTES  = (size_t)2 * NB * SQ * HD * 2;
constexpr size_t WS_WO_OFF   = WS_O_BYTES;

#define KV_AUX 2
#define O_AUX 0
#define GAP() __builtin_amdgcn_sched_barrier(0)
#define OPAQUE(x) asm volatile("" : "+v"(x))

#define ACLOB "a0","a1","a2","a3","a4","a5","a6","a7","a8","a9","a10","a11","a12","a13","a14","a15","a16","a17","a18","a19","a20","a21","a22","a23","a24","a25","a26","a27","a28","a29","a30","a31","a32","a33","a34","a35","a36","a37","a38","a39","a40","a41","a42","a43","a44","a45","a46","a47","a48","a49","a50","a51","a52","a53","a54","a55","a56","a57","a58","a59","a60","a61","a62","a63","a64","a65","a66","a67","a68","a69","a70","a71","a72","a73","a74","a75","a76","a77","a78","a79","a80","a81","a82","a83","a84","a85","a86","a87","a88","a89","a90","a91","a92","a93","a94","a95","a96","a97","a98","a99","a100","a101","a102","a103","a104","a105","a106","a107","a108","a109","a110","a111","a112","a113","a114","a115","a116","a117","a118","a119","a120","a121","a122","a123","a124","a125","a126","a127","a128","a129","a130","a131","a132","a133","a134","a135","a136","a137","a138","a139","a140","a141","a142","a143","a144","a145","a146","a147","a148","a149","a150","a151","a152","a153","a154","a155"

#define ACLOB2 "a156","a157","a158","a159","a160","a161","a162","a163","a164","a165","a166","a167","a168","a169","a170","a171","a172","a173","a174","a175","a176","a177","a178","a179","a180","a181","a182","a183","a184","a185","a186","a187","a188","a189","a190","a191","a192","a193","a194","a195","a196","a197","a198","a199","a200","a201","a202","a203","a204","a205","a206","a207","a208","a209","a210","a211","a212","a213","a214","a215","a216","a217","a218","a219","a220","a221","a222","a223","a224","a225","a226","a227"

DI unsigned cvtpk(float lo, float hi) {
  f32x2 v = {lo, hi}; bf16x2 b = __builtin_convertvector(v, bf16x2); return __builtin_bit_cast(unsigned, b);
}
DI float max3f(float a, float b, float c) { float r; asm("v_max3_f32 %0, %1, %2, %3" : "=v"(r) : "v"(a), "v"(b), "v"(c)); return r; }
DI float min3f(float a, float b, float c) { float r; asm("v_min3_f32 %0, %1, %2, %3" : "=v"(r) : "v"(a), "v"(b), "v"(c)); return r; }
DI float max2f(float a, float b) { float r; asm("v_max_f32_e32 %0, %1, %2" : "=v"(r) : "v"(a), "v"(b)); return r; }
DI float fsub_s(float a, float b) { float r; asm("v_sub_f32_e32 %0, %1, %2" : "=v"(r) : "v"(a), "v"(b)); return r; }
DI bf16x8 lds_b128(unsigned addr) { return *(const LAS bf16x8*)(uintptr_t)addr; }
DI bf16x4 lds_tr(unsigned a) { return __builtin_amdgcn_ds_read_tr16_b64_v4bf16((LAS bf16x4*)(uintptr_t)a); }
DI bf16x8 cat4(bf16x4 lo, bf16x4 hi) { return __builtin_shufflevector(lo, hi, 0, 1, 2, 3, 4, 5, 6, 7); }
DI float xmax16_32(float v) {
  auto r = __builtin_amdgcn_permlane16_swap(__float_as_uint(v), __float_as_uint(v), false, false);
  v = fmaxf(__uint_as_float(r[0]), __uint_as_float(r[1]));
  auto s = __builtin_amdgcn_permlane32_swap(__float_as_uint(v), __float_as_uint(v), false, false);
  return fmaxf(__uint_as_float(s[0]), __uint_as_float(s[1]));
}

constexpr int AO(int rt, int t) { return 4 * (13 * rt + t); }
template <int R, bool Z> DI void mfma_acc(const bf16x8& a, const bf16x8& b) {
  if constexpr (Z) asm volatile("v_mfma_f32_16x16x32_bf16 a[%c2:%c3], %0, %1, 0" :: "v"(a), "v"(b), "i"(R), "i"(R + 3));
  else             asm volatile("v_mfma_f32_16x16x32_bf16 a[%c2:%c3], %0, %1, a[%c2:%c3]" :: "v"(a), "v"(b), "i"(R), "i"(R + 3));
}
template <int R> DI float acc_read() { float r; asm volatile("v_accvgpr_read_b32 %0, a[%c1]" : "=v"(r) : "i"(R)); return r; }
template <int R> DI void acc_scale4(float f) {
  float t0, t1;
  asm volatile("v_accvgpr_read_b32 %0, a[%c3]\n\tv_accvgpr_read_b32 %1, a[%c4]\n\tv_mul_f32 %0, %2, %0\n\tv_mul_f32 %1, %2, %1\n\t"
               "v_accvgpr_write_b32 a[%c3], %0\n\tv_accvgpr_write_b32 a[%c4], %1\n\t"
               "v_accvgpr_read_b32 %0, a[%c5]\n\tv_accvgpr_read_b32 %1, a[%c6]\n\tv_mul_f32 %0, %2, %0\n\tv_mul_f32 %1, %2, %1\n\t"
               "v_accvgpr_write_b32 a[%c5], %0\n\tv_accvgpr_write_b32 a[%c6], %1"
               : "=&v"(t0), "=&v"(t1) : "v"(f), "i"(R), "i"(R + 1), "i"(R + 2), "i"(R + 3));
}

struct St {
  unsigned aX0, aX1, aK0, aK1, aW[4], aV[4];
  unsigned bX0, bX1, bW[4], bK0, bK1, bV[4];
  f32x4 qa[3][12];
  bf16x8 qf[3][6];
  f32x4 S[2][3][2];
  u32x4 P[2][3];
  bf16x4 flo[6], fhi[6];
  bf16x8 fk[6];
  bf16x8 xf[2][3];
  f32x4 negm4[3];
  float mx[3], fct[3];
  bf16x8 ones;
  bool pend;
  unsigned aflag, adone, flagv, step, rs0, rs1;
  float inv[3], et[8];
  u32x4 ew;
  __amdgpu_buffer_rsrc_t rO;
  unsigned voO;
};

#define STAMP(i)
enum { PH_QP = 0, PH_QK = 1, PH_PV = 2 };
constexpr int PD = 5;

template <int PH, int HALF, int F, int PART> DI void read_frag(St& c) {
  if constexpr (PH == PH_QP) {
    constexpr int ks = F / 12, t = F % 12;
    const unsigned a = c.bW[t & 3] + (t >> 2) * 128 + ks * (32 * 384);
    if constexpr (PART == 0) c.flo[F % 6] = lds_tr(a); else c.fhi[F % 6] = lds_tr(a + 4 * 384);
  } else if constexpr (PH == PH_QK) {
    constexpr int kt = HALF * 2 + F / 6, s = F % 6;
    if constexpr (PART == 0) c.fk[F % 6] = lds_b128(((s & 1) ? c.bK1 : c.bK0) + kt * 6144 + (s >> 1) * 128);
  } else {
    const unsigned a = c.bV[F & 3] + (F >> 2) * 128 + HALF * (32 * 384);
    if constexpr (PART == 0) c.flo[F % 6] = lds_tr(a); else c.fhi[F % 6] = lds_tr(a + 16 * 384);
  }
}
template <int KS, int RT> DI void read_x(St& c) {
  c.xf[KS][RT] = lds_b128((KS ? c.bX1 : c.bX0) + RT * 2048); }
DI unsigned vadd_s(unsigned sv, unsigned v) { unsigned r; asm volatile("v_add_u32 %0, %1, %2" : "=v"(r) : "s"(sv), "v"(v)); return r; }
template <unsigned C> DI unsigned vadd_c(unsigned v) { unsigned r; asm volatile("v_add_u32 %0, %1, %2" : "=v"(r) : "i"(C), "v"(v)); return r; }
DI void flag_read(St& c) { c.flagv = *(volatile LAS unsigned*)(uintptr_t)c.aflag; }
DI void flag_wait(St& c, unsigned need) {
  if (__builtin_expect(__builtin_amdgcn_readfirstlane(c.flagv) < need, 0)) {
    unsigned tv, ts;
    asm volatile("1:\n\tds_read_b32 %0, %2\n\ts_waitcnt lgkmcnt(0)\n\tv_readfirstlane_b32 %1, %0\n\ts_cmp_lt_u32 %1, %3\n\ts_cbranch_scc1 1b"
                 : "=&v"(tv), "=&s"(ts) : "v"(c.aflag), "s"(need) : "memory", "scc");
  }
  asm volatile("" ::: "memory");
}
DI void done_signal(St& c) {
  asm volatile("" ::: "memory");
  c.step = vadd_c<1>(c.step);
  *(volatile LAS unsigned*)(uintptr_t)c.adone = c.step;
}

constexpr int SM_NOPS = 13 + 36;
template <int X, bool FIRST, int I> DI void sm_op(St& c) {
  if constexpr (I < 12) {
    constexpr int kt2 = I / 6, rt = (I % 6) % 3, st = (I % 6) / 3;
    const f32x4& s = c.S[X][rt][kt2];
    if constexpr (kt2 == 0 && st == 0) c.mx[rt] = max3f(s[0], s[1], s[2]);
    else if constexpr (kt2 == 0)       c.mx[rt] = max2f(c.mx[rt], s[3]);
    else if constexpr (st == 0)        c.mx[rt] = max3f(c.mx[rt], s[0], s[1]);
    else                               c.mx[rt] = max3f(c.mx[rt], s[2], s[3]);
  } else if constexpr (I == 12) {
    bool bad = max3f(c.mx[0], c.mx[1], c.mx[2]) > THR;
    if constexpr (FIRST) bad = bad || (min3f(c.mx[0], c.mx[1], c.mx[2]) < -THR_LO);
    if (__builtin_expect(__any((int)bad), 0)) {
#pragma unroll
      for (int rt = 0; rt < 3; ++rt) {
        float d = xmax16_32(c.mx[rt]);
        if constexpr (!FIRST) d = fmaxf(d, 0.f);
        const float nm = c.negm4[rt][0] - d;
        c.negm4[rt] = f32x4{nm, nm, nm, nm}; OPAQUE(c.negm4[rt]);
#pragma unroll
        for (int kt2 = 0; kt2 < 2; ++kt2)
#pragma unroll
          for (int r = 0; r < 4; ++r) c.S[X][rt][kt2][r] = fsub_s(c.S[X][rt][kt2][r], d);
        c.fct[rt] = __builtin_amdgcn_exp2f(-d);
      }
      if constexpr (!FIRST) c.pend = true;
    }
  } else {
    constexpr int J = I - 13, rt = J / 12, k = J % 12;
    if constexpr (k < 8) {
      float e = __builtin_amdgcn_exp2f(c.S[X][rt][k >> 2][k & 3]); OPAQUE(e);
      c.S[X][rt][k >> 2][k & 3] = e;
    } else {
      constexpr int p = k - 8;
      unsigned w = cvtpk(c.S[X][rt][p >> 1][2 * (p & 1)], c.S[X][rt][p >> 1][2 * (p & 1) + 1]); OPAQUE(w);
      c.P[X][rt][p] = w;
    }
  }
}
template <int X, bool FIRST, int LO, int... Is> DI void sm_ops_(St& c, iseq<Is...>) { (sm_op<X, FIRST, LO + Is>(c), ...); }
template <int X, bool FIRST, int LO, int HI> DI void sm_ops(St& c) { if constexpr (HI > LO) sm_ops_<X, FIRST, LO>(c, make_iseq<HI - LO>{}); }

constexpr int sm_cost(int i) { return i < 12 ? 4 : (i == 12 ? 8 : (((i - 13) % 12) < 8 ? 8 : 4)); }
struct SmSched { int op[75][2]; int last; int pv_done[SM_NOPS]; };
constexpr SmSched make_sched() {
  SmSched s{};
  int at[SM_NOPS] = {};
  for (int i = 0; i < SM_NOPS; ++i) at[i] = -1;
  s.last = 0;
  for (int gi = 0; gi < 75; ++gi) {
    s.op[gi][0] = -1; s.op[gi][1] = -1;
    const bool pv = gi < 39;
    const int m = pv ? gi : gi - 39, grp = m / 3, r = m % 3;
    int cap = 8;
    if (pv) { if (grp < 12 - PD) { if (r < 2) cap = 4; } else if (grp < 12) { if (r == 0) cap = 4; } }
    else {
      if (grp < 12 - PD) { if (r == 0) cap = 4; } else { if (r < 2) cap = 4; }
      if (grp < 4 && r == 2) cap = 4;
      if (grp == 3 && r == 1) cap = 4;
      if (grp == 6 && r >= 1) cap = 0;
      if ((grp == 7 || grp == 8) && r == 2) cap = 4;
    }
    int n = 0;
    for (int pass = 0; pass < 2; ++pass)
      for (int i = 0; i < SM_NOPS && n < 2; ++i) {
        if (at[i] >= 0) continue;
        const int cst = sm_cost(i);
        if ((pass == 0) != (cst == 8) || cst > cap) continue;
        bool ok = true;
        if (i < 12) { if (i >= 3) ok = at[i - 3] >= 0 && at[i - 3] < gi; if (i >= 6 && gi < 4) ok = false; }
        else if (i == 12) { for (int k = 0; k < 12; ++k) ok = ok && at[k] >= 0 && at[k] < gi; }
        else {
          const int J = i - 13, rt = J / 12, k = J % 12;
          if (k < 8) ok = at[12] >= 0 && at[12] < gi;
          else { const int e0 = 13 + rt * 12 + 2 * (k - 8); ok = at[e0] >= 0 && at[e0] < gi && at[e0 + 1] >= 0 && at[e0 + 1] < gi; }
        }
        if (!ok) continue;
        at[i] = gi; s.op[gi][n++] = i; cap -= cst; s.last = gi;
      }
  }
  for (int i = 0; i < SM_NOPS; ++i) { s.pv_done[i] = (at[i] >= 0 && at[i] < 39) ? 1 : 0; if (at[i] < 0) s.last = 1000; }
  return s;
}
constexpr SmSched SCHED_N = make_sched();
static_assert(SCHED_N.last <= 72, "softmax schedule must finish before the last gaps of the QK phase");
template <int X, int... Is> DI void sm_rest_(St& c, iseq<Is...>) { ((SCHED_N.pv_done[Is] ? (void)0 : sm_op<X, false, Is>(c)), ...); }

template <int... Is> DI void rescale_(St& c, iseq<Is...>) { (acc_scale4<4 * Is>(c.fct[Is / 13]), ...); }
DI void rescale(St& c) {
  asm volatile("s_nop 7" ::: "memory");
  rescale_(c, make_iseq<39>{});
  asm volatile("s_nop 1" ::: "memory");
}

constexpr int EP_NOPS = 6 + 18 * 24;
constexpr int ASTASH = 156;
template <bool DIRECT, int I0> DI void ep_op(St& c) {
  if constexpr (I0 < 6) {
    constexpr int rt = I0 % 3;
    if constexpr (I0 < 3) c.inv[rt] = acc_read<AO(rt, 12)>();
    else { c.inv[rt] = __builtin_amdgcn_rcpf(c.inv[rt]); OPAQUE(c.inv[rt]); }
  } else {
    constexpr int I = I0 - 6, q = I / 24, k = I % 24, ES = q / 3, rt = q % 3;
    if constexpr (k < 8) c.et[k] = acc_read<AO(rt, 2 * ES + (k >> 2)) + (k & 3)>();
    else if constexpr (k < 16) { c.et[k - 8] = c.et[k - 8] * c.inv[rt]; OPAQUE(c.et[k - 8]); }
    else if constexpr (k < 20) { unsigned w = cvtpk(c.et[2 * (k - 16)], c.et[2 * (k - 16) + 1]); OPAQUE(w); c.ew[k - 16] = w; }
    else if constexpr (DIRECT) { if constexpr (k == 20) __builtin_amdgcn_raw_buffer_store_b128(c.ew, c.rO, c.voO, (ES * 9 + rt) * 1024, O_AUX); }
    else asm volatile("v_accvgpr_write_b32 a[%c1], %0" :: "v"(c.ew[k - 20]), "i"(ASTASH + 4 * q + (k - 20)));
  }
}
template <bool DIRECT, int LO, int... Is> DI void ep_ops_(St& c, iseq<Is...>) { (ep_op<DIRECT, LO + Is>(c), ...); }
template <bool DIRECT, int LO, int HI> DI void ep_ops(St& c) {
  constexpr int H2 = HI > EP_NOPS ? EP_NOPS : HI;
  if constexpr (H2 > LO) ep_ops_<DIRECT, LO>(c, make_iseq<H2 - LO>{});
}
template <int Q> DI void stash_store(St& c) {
  constexpr int ES = Q / 3, rt = Q % 3;
  asm volatile("buffer_store_dwordx4 a[%c3:%c4], %0, %1, %2 offen" :: "v"(c.voO), "s"(c.rO), "s"((unsigned)((ES * 9 + rt) * 1024)), "i"(ASTASH + 4 * Q), "i"(ASTASH + 4 * Q + 3) : "memory");
}

constexpr int ep_slot(int M) {
  int n = 0;
  for (int m = 0; m < M; ++m) {
    const int g = m / 3, r = m % 3;
    int cap = (r < 2) ? 1 : 2;
    if (r == 2 && g >= 4 && g < 7) cap = 1;
    if (r == 2 && g == 10) cap = 0;
    if (r == 2 && g == 13) cap = 1;
    if (r == 2 && g >= 15 && g <= 20) cap = 0;
    if (r == 2 && g == 21) cap = 1;
    if (r == 2 && g == 23) cap = 1;
    n += cap;
  }
  return n;
}
constexpr int EP_PER = ep_slot(72);
static_assert(EP_PER * 7 >= EP_NOPS, "epilogue ops must fit in 7 Q-projection steps");
template <int PH, int HALF, int X, int NPH, int NHALF, int NL0, int NL1, int ND, bool Z, int SM, int ES, int SQ_, int M>
DI void gap(St& c, unsigned nb) {
  constexpr bool SYNC = NL0 != -1, RTS = NL0 == -2;
  constexpr int NGF = (PH == PH_QP) ? 24 : 12;
  constexpr int g = M / 3, rt = M % 3;
  constexpr unsigned S0 = NL0 >= 0 ? (unsigned)(NL0 % 6) * SLOT : 0u, S1 = NL1 >= 0 ? (unsigned)(NL1 % 6) * SLOT : 0u;
#define ADD0(v) (RTS ? vadd_s(c.rs0, v) : vadd_c<S0>(v))
#define ADD1(v) (RTS ? vadd_s(c.rs1, v) : vadd_c<S1>(v))
  if constexpr (PH == PH_QP) {
    constexpr int ks = g / 12, t = g % 12;
    const bf16x8 f = cat4(c.flo[g % 6], c.fhi[g % 6]);
    if constexpr (Z && ks == 0) asm volatile("v_mfma_f32_16x16x32_bf16 %0, %1, %2, 0" : "=&v"(c.qa[rt][t]) : "v"(f), "v"(c.xf[ks][rt]));
    else                        asm volatile("v_mfma_f32_16x16x32_bf16 %0, %1, %2, %0" : "+v"(c.qa[rt][t]) : "v"(f), "v"(c.xf[ks][rt]));
  } else if constexpr (PH == PH_QK) {
    constexpr int kt2 = g / 6, s = g % 6;
    if constexpr (s == 0) asm volatile("v_mfma_f32_16x16x32_bf16 %0, %1, %2, %3" : "=&v"(c.S[X][rt][kt2]) : "v"(c.fk[g % 6]), "v"(c.qf[rt][s]), "v"(c.negm4[rt]));
    else                  asm volatile("v_mfma_f32_16x16x32_bf16 %0, %1, %2, %0" : "+v"(c.S[X][rt][kt2]) : "v"(c.fk[g % 6]), "v"(c.qf[rt][s]));
  } else {
    const bf16x8 pf = __builtin_bit_cast(bf16x8, c.P[X][rt]);
    if constexpr (g < 12) mfma_acc<AO(rt, g), Z>(cat4(c.flo[g % 6], c.fhi[g % 6]), pf);
    else                  mfma_acc<AO(rt, 12), Z>(c.ones, pf);
  }
  GAP();
  if constexpr (PH == PH_QK && M == 24) asm volatile("" :: "v"(c.negm4[0]), "v"(c.negm4[1]), "v"(c.negm4[2]));
  if constexpr (rt < 2) {
    if constexpr (g + PD < NGF) read_frag<PH, HALF, g + PD, rt>(c);
    else if constexpr (g + PD - NGF < PD) read_frag<NPH, NHALF, g + PD - NGF, rt>(c);
  }
  if constexpr (PH == PH_QP && g >= 4 && g < 7 && rt == 2) read_x<1, g - 4>(c);
  if constexpr (NPH == PH_QP && g == NGF - 5 && rt == 2) { read_x<0, 0>(c); read_x<0, 1>(c); }
  if constexpr (NPH == PH_QP && g == NGF - 4 && rt == 2) read_x<0, 2>(c);
  if constexpr (SYNC) {
    if constexpr (PH == PH_QK) {
      if constexpr (g == 3 && rt == 1) flag_read(c);
      if constexpr (g == 6 && rt == 1) flag_wait(c, nb + ND);
      if constexpr (g == 6 && rt == 2) done_signal(c);
      if constexpr (g < 4 && rt == 2) c.bV[g] = ADD1(c.aV[g]);
      if constexpr (g == 7 && rt == 2) c.bK0 = ADD0(c.aK0);
      if constexpr (g == 8 && rt == 2) c.bK1 = ADD0(c.aK1);
    } else if constexpr (PH == PH_QP) {
      if constexpr (g == 13 && rt == 2) flag_read(c);
      if constexpr (g == 17 && rt == 2) flag_wait(c, nb + ND);
      if constexpr (g == 18 && rt == 2) done_signal(c);
      if constexpr (NPH == PH_QK) {
        if constexpr (g == 10 && rt == 2) { c.bK0 = ADD0(c.aK0); c.bK1 = ADD0(c.aK1); }
      } else {
        if constexpr (g == 10 && rt == 2) { c.bX0 = ADD0(c.aX0); c.bX1 = ADD0(c.aX1); }
        if constexpr (g == 15 && rt == 2) c.bW[0] = ADD1(c.aW[0]);
        if constexpr (g == 16 && rt == 2) c.bW[1] = ADD1(c.aW[1]);
        if constexpr (g == 20 && rt == 2) c.bW[2] = ADD1(c.aW[2]);
        if constexpr (g == 21 && rt == 2) c.bW[3] = ADD1(c.aW[3]);
      }
    } else {
      if constexpr (g == 3 && rt == 2) flag_read(c);
      if constexpr (g == 5 && rt == 2) flag_wait(c, nb + ND);
      if constexpr (g == 6 && rt == 2) done_signal(c);
      if constexpr (g == 0 && rt == 2) { c.bX0 = ADD0(c.aX0); c.bX1 = ADD0(c.aX1); }
      if constexpr (g == 1 && rt == 2) { c.bW[0] = ADD1(c.aW[0]); c.bW[1] = ADD1(c.aW[1]); }
      if constexpr (g == 2 && rt == 2) { c.bW[2] = ADD1(c.aW[2]); c.bW[3] = ADD1(c.aW[3]); }
    }
  }
  if constexpr (SM == 1) {
    constexpr int gi = (PH == PH_PV) ? M : 39 + M;
    if constexpr (SCHED_N.op[gi][0] >= 0) sm_op<X ^ 1, false, SCHED_N.op[gi][0]>(c);
    if constexpr (SCHED_N.op[gi][1] >= 0) sm_op<X ^ 1, false, SCHED_N.op[gi][1]>(c);
  } else if constexpr (SM == 2) {
    if constexpr (M >= 24 && M < 30) sm_ops<X, true, M - 24, M - 23>(c);
  } else if constexpr (SM == 3) {
    if constexpr (M < 34) sm_ops<X ^ 1, true, 13 + M, 14 + M>(c);
    if constexpr (M == 33) sm_ops<X ^ 1, true, 13 + 34, 13 + 36>(c);
  }
  if constexpr (ES >= 0) {
    constexpr int e0 = ES * EP_PER + ep_slot(M), e1 = ES * EP_PER + ep_slot(M + 1);
    ep_ops<false, e0, e1>(c);
  }
  if constexpr (SQ_ >= 0 && M == (PH == PH_QP ? 71 : (PH == PH_QK ? 35 : 38))) stash_store<SQ_>(c);
  GAP();
}
template <int PH, int HALF, int X, int NPH, int NHALF, int NL0, int NL1, int ND, bool Z, int SM, int ES, int SQ_, int... Ms>
DI void phase_(St& c, unsigned nb, iseq<Ms...>) { (gap<PH, HALF, X, NPH, NHALF, NL0, NL1, ND, Z, SM, ES, SQ_, Ms>(c, nb), ...); }
template <int PH, int HALF, int X, int NPH, int NHALF, int NL0, int NL1, int ND, bool Z, int SM, int ES = -1, int SQ_ = -1>
DI void phase(St& c, unsigned nb) {
  constexpr int NM = (PH == PH_QP) ? 72 : (PH == PH_QK ? 36 : 39);
  phase_<PH, HALF, X, NPH, NHALF, NL0, NL1, ND, Z, SM, ES, SQ_>(c, nb, make_iseq<NM>{});
  if constexpr (PH == PH_PV && SM == 1) {
    if (__builtin_expect(c.pend, 0)) { rescale(c); c.pend = false; }
    GAP();
  }
}

template <int RT, int S> DI void q_convert(St& c) {
  const f32x4 a = c.qa[RT][2 * S] * QSCALE, b = c.qa[RT][2 * S + 1] * QSCALE;
  unsigned pa0 = cvtpk(a[0], a[1]), pa1 = cvtpk(a[2], a[3]);
  unsigned pb0 = cvtpk(b[0], b[1]), pb1 = cvtpk(b[2], b[3]);
  auto r0 = __builtin_amdgcn_permlane32_swap(pa0, pb0, false, false);
  auto r1 = __builtin_amdgcn_permlane32_swap(pa1, pb1, false, false);
  auto t0 = __builtin_amdgcn_permlane16_swap(r0[0], r0[1], false, false);
  auto t1 = __builtin_amdgcn_permlane16_swap(r1[0], r1[1], false, false);
  u32x4 w = {t0[0], t1[0], t0[1], t1[1]};
  c.qf[RT][S] = __builtin_bit_cast(bf16x8, w);
}
template <int... Is> DI void q_convert_all(St& c, iseq<Is...>) { (q_convert<Is % 3, Is / 3>(c), ...); }

__host__ __device__ constexpr unsigned slot_of(int n) { return (unsigned)(n % 6) * SLOT; }
__host__ __device__ constexpr int kload(int j) { return j == 0 ? 20 : 2 * j + 19; }
__host__ __device__ constexpr int vload(int j) { return j == 7 ? 35 : 2 * j + 22; }

struct Ld {
  __amdgpu_buffer_rsrc_t rX, rK, rV, rW;
  unsigned vK[6], vV[3], vW[12], vX[4], vL[3];
  unsigned ldsb, sW[2], sX[2], sK[2];
  u32x4 buf[2][24];
  __amdgpu_buffer_rsrc_t rWO, rWF;
  unsigned sWO, sWF, vWO, vWA, vWR, vWF;
  unsigned base_step, base_load;
};
#define DMA16I(rsrc, ldsoff, voff, soff, imm, aux) \
  __builtin_amdgcn_raw_ptr_buffer_load_lds((rsrc), (LAS void*)(uintptr_t)(ldsoff), 16, (voff), (soff), (imm), (aux))
constexpr int c_hi(int T) { return T < 0 ? (T == -1 ? 0 : -1) : (T < 10 ? 2 * T + 2 : (T == 10 ? 21 : (T < 18 ? 2 * T + 1 : (T == 18 ? 36 : 38)))); }
#define NFIFO 8
static_assert(NFIFO <= 8, "FIFO entry 2 is reused by the wo re-layout from step 13 on");
constexpr int xw_instr(int N) { return ((N % 36) & 1) == 0 ? 18 : 24; }
struct LSeq { int id[96]; int cnt[96]; int step[96]; int n; };
constexpr int kv_chunks_at(int T, int k) {
  if (T == 17) return k < 2 ? k : -1;
  if (T == 18) return k < 2 ? 2 + k : -1;
  if (T < 8) return k < 1 ? 4 + T : -1;
  return -1;
}
constexpr LSeq make_lseq() {
  LSeq q{}; q.n = 0;
  for (int T = 0; T < 19; ++T) {
    const int lo = c_hi(T - 2) + 6, hi = c_hi(T - 1) + 6;
    for (int n = lo; n < hi; ++n) {
      if ((n % 36) < 20) { q.id[q.n] = n; q.cnt[q.n] = xw_instr(n); q.step[q.n] = T; ++q.n; }
      else if (n - 20 >= NFIFO) { q.id[q.n] = n; q.cnt[q.n] = 24; q.step[q.n] = T; ++q.n; }
      else { const int i = n - 20; if (i + 4 < NFIFO) for (int cch = 0; cch < 3; ++cch) { q.id[q.n] = 100 + 3 * (i + 4) + cch; q.cnt[q.n] = 8; q.step[q.n] = T; ++q.n; } }
    }
    if (T == 13) { q.id[q.n] = 300; q.cnt[q.n] = 20; q.step[q.n] = T; ++q.n; }
    if (T == 15) { q.id[q.n] = 301; q.cnt[q.n] = 20; q.step[q.n] = T; ++q.n; }
    for (int k = 0; k < 2; ++k) {
      const int ch = kv_chunks_at(T, k);
      if (ch >= 0) { q.id[q.n] = 100 + (T >= 17 ? 48 : 0) + ch; q.cnt[q.n] = 8; q.step[q.n] = T; ++q.n; }
    }
  }
  return q;
}
constexpr LSeq LSEQ = make_lseq();
constexpr int lseq_pos(int id) { for (int k = 0; k < LSEQ.n; ++k) if (LSEQ.id[k] == id) return k; return -1; }
constexpr int younger(int id, int cur) {
  int p = lseq_pos(id);
  int r = 0;
  if (p >= 0 && p < cur) { for (int k = p + 1; k < cur; ++k) r += LSEQ.cnt[k]; return r; }
  const int pid = id < 100 ? id + 36 : id + 48;
  p = lseq_pos(pid);
  for (int k = p + 1; k < LSEQ.n; ++k) r += LSEQ.cnt[k];
  for (int k = 0; k < cur; ++k) r += LSEQ.cnt[k];
  return r;
}
template <int N> DI void vm_wait() { asm volatile("s_waitcnt vmcnt(%0)" :: "i"(N > 63 ? 63 : N) : "memory"); }

template <int N, int P> DI void xw_piece(Ld& ld) {
  constexpr int n = N % 36, nx = N / 36;
  const unsigned l0 = ld.ldsb + slot_of(n) + (P >> 2) * 4096;
  constexpr int imm = (P & 3) * 1024;
  if constexpr ((n & 1) == 0) DMA16I(ld.rX, l0, ld.vX[P & 3], ld.sX[nx] + (n >> 1) * 128 + (P >> 2) * (32 * DM * 2), imm, 0);
  else                        DMA16I(ld.rW, l0, ld.vW[P % 12], ld.sW[nx] + (unsigned)((n >> 1) * 64 + (P / 12) * 32) * (HD * 2), imm, 0);
}
template <int N, int... Ps> DI void xw_load_(Ld& ld, iseq<Ps...>) { (xw_piece<N, Ps>(ld), ...); }
template <int N> DI void xw_load(Ld& ld) {
  xw_load_<N>(ld, make_iseq<xw_instr(N)>{}); }

constexpr bool kv_isK(int i) { return i == 0 || (i < 15 && (i & 1)); }
constexpr int kv_j(int i) { return kv_isK(i) ? (i + 1) >> 1 : (i == 15 ? 7 : (i - 2) >> 1); }
template <int I, int P> DI void kv_issue_piece(Ld& ld) {
  constexpr int i = I % 16, nx = I / 16, e = i & 3, R = 96 * e + 4 * P;
  const unsigned so = ld.sK[nx] + kv_j(i) * SLOT + (P >> 2) * 4096;
  const unsigned vo = kv_isK(i) ? ld.vK[P % 6] : ld.vV[P % 3];
  const __amdgpu_buffer_rsrc_t rs = kv_isK(i) ? ld.rK : ld.rV;
  if constexpr (e < 2) asm volatile("buffer_load_dwordx4 a[%c3:%c4], %0, %1, %2 offen offset:%c5 nt" :: "v"(vo), "s"(rs), "s"(so), "i"(R), "i"(R + 3), "i"((P & 3) * 1024) : "memory");
  else                 asm volatile("buffer_load_dwordx4 %0, %1, %2, %3 offen offset:%c4 nt" : "=v"(ld.buf[e - 2][P]) : "v"(vo), "s"(rs), "s"(so), "i"((P & 3) * 1024) : "memory");
}
template <int I, int P0, int... Ps> DI void kv_issue_(Ld& ld, iseq<Ps...>) { (kv_issue_piece<I, P0 + Ps>(ld), ...); }
template <int I, int CH> DI void kv_chunk(Ld& ld) { kv_issue_<I, 8 * CH>(ld, make_iseq<8>{}); }
template <int I, int P> DI void kv_write_piece(Ld& ld) {
  constexpr int e = I & 3, R = 96 * e + 4 * P, sl = (20 + I) % 6;
  constexpr int off = (sl & 1) * SLOT + P * 1024;
  if constexpr (e < 2) asm volatile("ds_write_b128 %0, a[%c1:%c2] offset:%c3" :: "v"(ld.vL[sl >> 1]), "i"(R), "i"(R + 3), "i"(off) : "memory");
  else                 asm volatile("ds_write_b128 %0, %1 offset:%c2" :: "v"(ld.vL[sl >> 1]), "v"(ld.buf[e - 2][P]), "i"(off) : "memory");
}
template <int I, int... Ps> DI void kv_write_(Ld& ld, iseq<Ps...>) { (kv_write_piece<I, Ps>(ld), ...); }

DI void publish(Ld& ld, unsigned v) {
  asm volatile("ds_write_b32 %0, %1" :: "v"(ld.ldsb + FLAG_OFF), "v"(v) : "memory");
}
DI void wait_done(Ld& ld, unsigned need) {
  while (true) {
    u32x4 d;
    asm volatile("ds_read_b128 %0, %1\n\ts_waitcnt lgkmcnt(0)" : "=v"(d) : "v"(ld.ldsb + FLAG_OFF + 16) : "memory");
    const unsigned m = __builtin_amdgcn_readfirstlane(min(min(d[0], d[1]), d[2]));
    if (m >= need) break;
    __builtin_amdgcn_s_sleep(1);
  }
}
constexpr int lseq_before(int T, int n) {
  int k = 0;
  while (k < LSEQ.n && LSEQ.step[k] < T) ++k;
  const int lo = c_hi(T - 2) + 6, hi = c_hi(T - 1) + 6;
  for (int m = lo; m < hi && m < n; ++m) { if ((m % 36) < 20 || m - 20 >= NFIFO) k += 1; else if (m - 20 + 4 < NFIFO) k += 3; }
  return k;
}
template <int I, int P> DI void kv_dma_piece(Ld& ld) {
  const unsigned l0 = ld.ldsb + slot_of(20 + I) + (P >> 2) * 4096;
  constexpr int imm = (P & 3) * 1024;
  if constexpr (kv_isK(I)) DMA16I(ld.rK, l0, ld.vK[P % 6], ld.sK[0] + kv_j(I) * SLOT + (P >> 2) * 4096, imm, KV_AUX);
  else                     DMA16I(ld.rV, l0, ld.vV[P % 3], ld.sK[0] + kv_j(I) * SLOT + (P >> 2) * 4096, imm, KV_AUX);
}
template <int I, int... Ps> DI void kv_dma_(Ld& ld, iseq<Ps...>) { (kv_dma_piece<I, Ps>(ld), ...); }
template <int T, int N> DI void loader_load(Ld& ld) {
  if constexpr ((N % 36) < 20) xw_load<N>(ld);
  else if constexpr (N - 20 >= NFIFO) {
    kv_dma_<N - 20>(ld, make_iseq<24>{});
  } else {
    constexpr int i = N - 20;
    vm_wait<younger(100 + 3 * i + 2, lseq_before(T, N))>();
    kv_write_<i>(ld, make_iseq<24>{});
    asm volatile("s_waitcnt lgkmcnt(0)" ::: "memory");
    publish(ld, ld.base_load + N + 1);
    if constexpr (i + 4 < NFIFO) { kv_chunk<i + 4, 0>(ld); kv_chunk<i + 4, 1>(ld); kv_chunk<i + 4, 2>(ld); }
  }
}
template <int I> DI void wo_load(Ld& ld) {
  asm volatile("buffer_load_dwordx4 %0, %1, %2, %3 offen offset:%c4" : "=v"(ld.buf[0][I]) : "v"(ld.vWO), "s"(ld.rWO), "s"(ld.sWO + (I & 3) * (8 * DM * 2)), "i"((I >> 2) * 128) : "memory");
}
template <int... Is> DI void wo_loads(Ld& ld, iseq<Is...>) { (wo_load<Is>(ld), ...); }
template <int CH> DI void wo_chunk(Ld& ld) {
  u32x2 a0, a1, a2, a3, b0, b1, b2, b3;
  asm volatile("ds_write_b128 %8, %10\n\tds_write_b128 %8, %11 offset:1280\n\tds_write_b128 %8, %12 offset:2560\n\tds_write_b128 %8, %13 offset:3840\n\t"
               "ds_read_b64_tr_b16 %0, %9\n\tds_read_b64_tr_b16 %4, %9 offset:2560\n\t"
               "ds_read_b64_tr_b16 %1, %9 offset:32\n\tds_read_b64_tr_b16 %5, %9 offset:2592\n\t"
               "ds_read_b64_tr_b16 %2, %9 offset:64\n\tds_read_b64_tr_b16 %6, %9 offset:2624\n\t"
               "ds_read_b64_tr_b16 %3, %9 offset:96\n\tds_read_b64_tr_b16 %7, %9 offset:2656\n\t"
               "s_waitcnt lgkmcnt(0)"
               : "=&v"(a0), "=&v"(a1), "=&v"(a2), "=&v"(a3), "=&v"(b0), "=&v"(b1), "=&v"(b2), "=&v"(b3)
               : "v"(ld.vWA), "v"(ld.vWR), "v"(ld.buf[0][CH * 4]), "v"(ld.buf[0][CH * 4 + 1]), "v"(ld.buf[0][CH * 4 + 2]), "v"(ld.buf[0][CH * 4 + 3]) : "memory");
#define WO_ST(t, lo, hi) { constexpr int nt = CH * 4 + t; u32x4 w = {lo[0], lo[1], hi[0], hi[1]}; \
    __builtin_amdgcn_raw_buffer_store_b128(w, ld.rWF, ld.vWF, ld.sWF + ((nt / 5) * 192 * 5 + nt % 5) * 1024, 0); }
  WO_ST(0, a0, b0) WO_ST(1, a1, b1) WO_ST(2, a2, b2) WO_ST(3, a3, b3)
#undef WO_ST
}
template <int T, int LO, int... Is> DI void loader_loads_(Ld& ld, iseq<Is...>) { (loader_load<T, LO + Is>(ld), ...); }
template <int T> DI void loader_step(Ld& ld) {
  constexpr int lo = c_hi(T - 2) + 6, hi = c_hi(T - 1) + 6, need = c_hi(T + 1);
  asm volatile("" : "+s"(ld.ldsb), "+s"(ld.sW[0]), "+s"(ld.sX[0]), "+s"(ld.sK[0]));
  wait_done(ld, ld.base_step + T);
  if constexpr (T == 8) {
    vm_wait<younger(19, lseq_before(T, 0))>();
    publish(ld, ld.base_load + 20);
  }
  loader_loads_<T, lo>(ld, make_iseq<hi - lo>{});
  if constexpr (T != 8 && (need <= 20 || need - 1 - 20 >= NFIFO)) {
    vm_wait<younger(need - 1, lseq_before(T, 1000))>();
    publish(ld, ld.base_load + need);
  }
  if constexpr (T == 13) wo_loads(ld, make_iseq<20>{});
  if constexpr (T == 15) {
    vm_wait<younger(300, lseq_before(T, 1000))>();
    wo_chunk<0>(ld); wo_chunk<1>(ld); wo_chunk<2>(ld); wo_chunk<3>(ld); wo_chunk<4>(ld);
    asm volatile("" ::: "memory");
  }
  if constexpr (kv_chunks_at(T, 0) >= 0) { constexpr int ch = kv_chunks_at(T, 0); kv_chunk<(T >= 17 ? 16 : 0) + ch / 3, ch % 3>(ld); }
  if constexpr (kv_chunks_at(T, 1) >= 0) { constexpr int ch = kv_chunks_at(T, 1); kv_chunk<(T >= 17 ? 16 : 0) + ch / 3, ch % 3>(ld); }
}
template <int... Ts> DI void loader_unit(Ld& ld, iseq<Ts...>) { (loader_step<Ts>(ld), ...); }

__global__ __launch_bounds__(256, 1)
void attn_fused_kernel(const uint16_t* __restrict__ xA, const uint16_t* __restrict__ kA, const uint16_t* __restrict__ vA,
                       const uint16_t* __restrict__ wqA, const uint16_t* __restrict__ woA,
                       const uint16_t* __restrict__ xB, const uint16_t* __restrict__ kB, const uint16_t* __restrict__ vB,
                       const uint16_t* __restrict__ wqB, const uint16_t* __restrict__ woB,
                       uint16_t* __restrict__ Ows, uint16_t* __restrict__ WOws) {
  extern __shared__ __attribute__((aligned(16))) char smem[];
  const unsigned ldsb = (unsigned)(uintptr_t)(LAS char*)smem;
  const int tid = threadIdx.x;
  const int wave = __builtin_amdgcn_readfirstlane(tid >> 6);
  const int l = tid & 63;
  const int wg = blockIdx.x;
  const int xcd = wg & 7, wi = wg >> 3;
  const int blk = xcd >> 2;
  const int head = (xcd & 3) * 8 + (wi & 7);
  const int bsub = wi >> 3;

  if (tid < 16) *(volatile LAS unsigned*)(uintptr_t)(ldsb + FLAG_OFF + tid * 4) = 0u;
  asm volatile("s_waitcnt lgkmcnt(0)\n\ts_barrier" ::: "memory");

  if (wave == 3) {
    const uint16_t* xp = blk ? xB : xA; const uint16_t* kp = blk ? kB : kA;
    const uint16_t* vp = blk ? vB : vA; const uint16_t* wqp = blk ? wqB : wqA;
    Ld ld;
    asm volatile("" ::: "a191");
    ld.ldsb = ldsb;
    const unsigned l3 = l >> 3, l7 = l & 7;
#pragma unroll
    for (int p = 0; p < 12; ++p) {
      const unsigned line = 8 * p + l3, row = line / 3, seg = line % 3;
      const unsigned f = (((row >> 1) & 1) | ((row >> 2) & 2)) << 1;
      ld.vW[p] = row * (HD * 2) + seg * 128 + ((l7 ^ f) << 4) - (p & 3) * 1024;
      if (p < 6) ld.vK[p] = l3 * 128 + ((l7 ^ ((row >> 1) & 7)) << 4);
      if (p < 3) ld.vV[p] = l3 * 128 + ((l7 ^ (((row >> 1) & 3) << 1)) << 4);
      if (p < 4) ld.vX[p] = line * (DM * 2) + ((l7 ^ ((line >> 1) & 7)) << 4) - p * 1024;
      if (p < 3) ld.vL[p] = ldsb + p * 2 * SLOT + l * 16;
    }
    ld.sW[0] = ld.sW[1] = head * (DH * 2);
    ld.rX = __builtin_amdgcn_make_buffer_rsrc((void*)xp, 0, NB * SQ * DM * 2, 0x00020000);
    ld.rK = __builtin_amdgcn_make_buffer_rsrc((void*)kp, 0, NB * NH * SK * DH * 2, 0x00020000);
    ld.rV = __builtin_amdgcn_make_buffer_rsrc((void*)vp, 0, NB * NH * SK * DH * 2, 0x00020000);
    ld.rW = __builtin_amdgcn_make_buffer_rsrc((void*)wqp, 0, DM * HD * 2, 0x00020000);
    ld.sX[1] = (unsigned)(bsub * SQ) * (DM * 2);
    ld.sK[1] = (unsigned)((bsub * NH + head) * SK) * (DH * 2);
    const int rb = wg >> 1, nh = wg & 1, b2 = rb >> 6, ks0 = (rb & 63) * 3;
    ld.rWO = __builtin_amdgcn_make_buffer_rsrc((void*)(b2 ? woB : woA), 0, HD * DM * 2, 0x00020000);
    ld.rWF = __builtin_amdgcn_make_buffer_rsrc((void*)WOws, 0, 2 * HD * DM * 2, 0x00020000);
    ld.vWO = l3 * (DM * 2) + l7 * 16;
    ld.vWA = ldsb + SCR_OFF + l3 * 160 + l7 * 16;
    ld.vWR = ldsb + SCR_OFF + (4 * (l >> 4) + ((l >> 2) & 3)) * 160 + (l & 3) * 8;
    ld.vWF = l * 16;
    ld.base_step = 0;
    xw_load<36>(ld);
    xw_load<37>(ld); xw_load<38>(ld); kv_chunk<16, 0>(ld); kv_chunk<16, 1>(ld);
    xw_load<39>(ld); xw_load<40>(ld);
    vm_wait<younger(37, lseq_before(18, 1000))>();
    publish(ld, 2);
    kv_chunk<16, 2>(ld); kv_chunk<17, 0>(ld);
    for (int u = 0; u < 4; ++u) {
      ld.base_step = 19 * u; ld.base_load = 36 * u;
      ld.sX[0] = ld.sX[1]; ld.sK[0] = ld.sK[1];
      ld.sX[1] = (u < 3) ? ld.sX[0] + 4 * SQ * (DM * 2) : 0x40000000u;
      ld.sK[1] = (u < 3) ? ld.sK[0] + 4 * NH * SK * (DH * 2) : 0x40000000u;
      ld.sW[1] = (u < 3) ? ld.sW[0] : 0x40000000u;
      ld.sWO = (u < 3) ? (unsigned)(32 * (ks0 + u)) * (DM * 2) + nh * 640 : 0x40000000u;
      ld.sWF = (u < 3) ? (unsigned)(((b2 * 8 + nh * 4) * 192 + ks0 + u) * 5) * 1024 : 0x40000000u;
      asm volatile("" ::: ACLOB, ACLOB2);
      loader_unit(ld, make_iseq<19>{});
    }
    asm volatile("s_waitcnt vmcnt(0)" ::: "memory");
    return;
  }

  const int cc = l & 15, g = l >> 4, q4 = (l >> 2) & 3, p4 = l & 3;
  St c;
  {
    const unsigned swz8 = (cc >> 1) & 7;
    c.aX0 = ldsb + (48 * wave + cc) * 128 + ((g ^ swz8) << 4);
    c.aX1 = ldsb + (48 * wave + cc) * 128 + (((4 + g) ^ swz8) << 4);
    c.aK0 = ldsb + cc * 384 + ((g ^ swz8) << 4);
    c.aK1 = ldsb + cc * 384 + (((4 + g) ^ swz8) << 4);
    const unsigned fp = ((q4 >> 1) & 1) | ((g & 1) << 1);
#pragma unroll
    for (int i = 0; i < 4; ++i) {
      const unsigned inrow = ((i ^ fp) << 5) + ((p4 >> 1) << 4) + ((p4 & 1) << 3);
      c.aW[i] = ldsb + (8 * g + q4) * 384 + inrow;
      c.aV[i] = ldsb + (4 * g + q4) * 384 + inrow;
    }
    u32x4 o1 = {0x3F803F80u, 0x3F803F80u, 0x3F803F80u, 0x3F803F80u};
    c.ones = __builtin_bit_cast(bf16x8, o1); OPAQUE(c.ones);
    c.pend = false;
    c.aflag = ldsb + FLAG_OFF; c.adone = ldsb + FLAG_OFF + 16 + wave * 4; c.step = 0;
    OPAQUE(c.aflag); OPAQUE(c.adone); OPAQUE(c.step);
    c.voO = l * 16;
    c.rO = __builtin_amdgcn_make_buffer_rsrc((void*)Ows, 0, 0, 0x00020000);
    asm volatile("" ::: ACLOB, ACLOB2);
  }

  flag_read(c); flag_wait(c, 2);
  c.bX0 = vadd_c<slot_of(0)>(c.aX0); c.bX1 = vadd_c<slot_of(0)>(c.aX1);
  c.bW[0] = vadd_c<slot_of(1)>(c.aW[0]); c.bW[1] = vadd_c<slot_of(1)>(c.aW[1]); c.bW[2] = vadd_c<slot_of(1)>(c.aW[2]); c.bW[3] = vadd_c<slot_of(1)>(c.aW[3]);
  read_frag<PH_QP, 0, 0, 0>(c); read_frag<PH_QP, 0, 0, 1>(c);
  read_x<0, 0>(c); read_x<0, 1>(c); read_x<0, 2>(c);
  read_frag<PH_QP, 0, 1, 0>(c); read_frag<PH_QP, 0, 1, 1>(c);
  read_frag<PH_QP, 0, 2, 0>(c); read_frag<PH_QP, 0, 2, 1>(c);
  read_frag<PH_QP, 0, 3, 0>(c); read_frag<PH_QP, 0, 3, 1>(c);
  read_frag<PH_QP, 0, 4, 0>(c); read_frag<PH_QP, 0, 4, 1>(c);

  for (int u = 0; u < 4; ++u) {
    const int batch = 4 * u + bsub;
    const unsigned nb = 36 * u;
    asm volatile("" ::: ACLOB, ACLOB2);
    GAP();
    STAMP(0);
    phase<PH_QP, 0, 0, PH_QP, 0,  2,  3,  4, true,  0, 0, 0>(c, nb);
    phase<PH_QP, 0, 0, PH_QP, 0,  4,  5,  6, false, 0, 1, 1>(c, nb);
    phase<PH_QP, 0, 0, PH_QP, 0,  6,  7,  8, false, 0, 2, 2>(c, nb);
    phase<PH_QP, 0, 0, PH_QP, 0,  8,  9, 10, false, 0, 3, 3>(c, nb);
    phase<PH_QP, 0, 0, PH_QP, 0, 10, 11, 12, false, 0, 4, 4>(c, nb);
    phase<PH_QP, 0, 0, PH_QP, 0, 12, 13, 14, false, 0, 5, 5>(c, nb);
    STAMP(1);
    phase<PH_QP, 0, 0, PH_QP, 0, 14, 15, 16, false, 0, 6, 6>(c, nb);
    phase<PH_QP, 0, 0, PH_QP, 0, 16, 17, 18, false, 0, -1, 7>(c, nb);
    phase<PH_QP, 0, 0, PH_QP, 0, 18, 19, 20, false, 0, -1, 8>(c, nb);
    STAMP(2);
    phase<PH_QP, 0, 0, PH_QK, 0, kload(0), -1, 21, false, 0, -1, 9>(c, nb);
    STAMP(10);
    asm volatile("s_nop 7" ::: "memory");
    GAP();
    q_convert_all(c, make_iseq<18>{});
#pragma unroll
    for (int rt = 0; rt < 3; ++rt) { c.negm4[rt] = f32x4{0.f, 0.f, 0.f, 0.f}; OPAQUE(c.negm4[rt]); }
    GAP();
    asm volatile("s_nop 1" ::: "memory");
    GAP();
    STAMP(11);
    phase<PH_QK, 0, 0, PH_QK, 1, -1, -1, 0, false, 2, -1, 10>(c, nb);
    asm volatile("s_nop 7" ::: "memory");
    GAP();
    sm_ops<0, true, 6, 13>(c);
    GAP();
    asm volatile("s_nop 1" ::: "memory");
    GAP();
    phase<PH_QK, 1, 1, PH_PV, 0, kload(1), vload(0), 23, false, 3, -1, 11>(c, nb);
    phase<PH_PV, 0, 0, PH_QK, 0, -1, -1, 0, true, 1, -1, 12>(c, nb);
    phase<PH_QK, 0, 0, PH_PV, 1, -1, -1, 0, false, 1, -1, 13>(c, nb);
    phase<PH_PV, 1, 1, PH_QK, 1, -1, -1, 0, false, 1, -1, 14>(c, nb);
    phase<PH_QK, 1, 1, PH_PV, 0, kload(2), vload(1), 25, false, 1, -1, 15>(c, nb);
    c.rs0 = slot_of(25);
    for (unsigned nd = nb + 27; nd != nb + 39; nd += 2) {
      const bool lastj = nd == nb + 37;
      c.rs1 = lastj ? c.rs0 : ((c.rs0 == 5 * SLOT) ? 0u : c.rs0 + SLOT);
      phase<PH_PV, 0, 0, PH_QK, 0, -1, -1, 0, false, 1>(c, nb);
      phase<PH_QK, 0, 0, PH_PV, 1, -1, -1, 0, false, 1>(c, nb);
      phase<PH_PV, 1, 1, PH_QK, 1, -1, -1, 0, false, 1>(c, nb);
      phase<PH_QK, 1, 1, PH_PV, 0, -2, -2, 0, false, 1>(c, lastj ? nb + 36 : nd);
      c.rs0 = (c.rs0 == 5 * SLOT) ? SLOT : c.rs0 + 2 * SLOT;
    }
    phase<PH_PV, 0, 0, PH_PV, 1, -1, -1, 0, false, 1, -1, 16>(c, nb);
    sm_rest_<1>(c, make_iseq<SM_NOPS>{});
    GAP();
    asm volatile("s_nop 1" ::: "memory");
    GAP();
    phase<PH_PV, 1, 1, PH_QP, 0, 0, 1, 38, false, 0, -1, 17>(c, nb);
    STAMP(39);
    c.rO = __builtin_amdgcn_make_buffer_rsrc((void*)(Ows + ((size_t)((blk * NB + batch) * 192 + head * 6) * 9 + wave * 3) * 512), 0, 48 * 1024, 0x00020000);
    asm volatile("s_nop 7" ::: "memory");
    STAMP(40);
  }
  GAP();
  ep_ops<true, 0, EP_NOPS>(c);
}

constexpr unsigned LDS2_BYTES = 45 * 3 * 1024;
struct Op {
  u32x4 of[4][9], wf[4][5];
  __amdgpu_buffer_rsrc_t rO, rW;
  unsigned vo[9];
};
template <int ST, int I> DI void op_load(Op& o, unsigned koff) {
  if constexpr (I < 5) o.wf[ST][I] = __builtin_amdgcn_raw_buffer_load_b128(o.rW, o.vo[I], koff * 5, 0);
  else                 o.of[ST][I - 5] = __builtin_amdgcn_raw_buffer_load_b128(o.rO, o.vo[I - 5], koff * 9, 0);
}
template <int ST, bool Z, int M> DI void op_gap(Op& o, unsigned koff_next) {
  constexpr int rt = M / 5, nt = M % 5, R = 4 * M;
  if constexpr (Z) asm volatile("v_mfma_f32_16x16x32_bf16 a[%c2:%c3], %0, %1, 0" :: "v"(o.wf[ST][nt]), "v"(o.of[ST][rt]), "i"(R), "i"(R + 3));
  else             asm volatile("v_mfma_f32_16x16x32_bf16 a[%c2:%c3], %0, %1, a[%c2:%c3]" :: "v"(o.wf[ST][nt]), "v"(o.of[ST][rt]), "i"(R), "i"(R + 3));
  GAP();
  if constexpr (M % 3 == 1 && M / 3 < 14) op_load<(ST + 3) % 4, M / 3>(o, koff_next);
  GAP();
}
template <int ST, bool Z, int... Ms> DI void op_step_(Op& o, unsigned koff_next, iseq<Ms...>) { (op_gap<ST, Z, Ms>(o, koff_next), ...); }
template <int ST, bool Z> DI void op_step(Op& o, unsigned koff_next) { op_step_<ST, Z>(o, koff_next, make_iseq<45>{}); }
template <int ST, int... Is> DI void op_load_all(Op& o, unsigned koff, iseq<Is...>) { (op_load<ST, Is>(o, koff), ...); }
template <int I> DI f32x4 acc_read4() { f32x4 v; v[0] = acc_read<4 * I>(); v[1] = acc_read<4 * I + 1>(); v[2] = acc_read<4 * I + 2>(); v[3] = acc_read<4 * I + 3>(); return v; }
template <int W, int I> DI void op_red_write(unsigned rb) {
  constexpr int own = I & 3;
  if constexpr (own != W) { constexpr int slot = (W < own) ? W : W - 1; *(LAS f32x4*)(uintptr_t)(rb + (I * 3 + slot) * 1024) = acc_read4<I>(); }
}
template <int W, int I> DI void op_red_final(unsigned rb, uint16_t* outp) {
  if constexpr ((I & 3) == W) {
    f32x4 v = acc_read4<I>();
    v += *(const LAS f32x4*)(uintptr_t)(rb + (I * 3 + 0) * 1024);
    v += *(const LAS f32x4*)(uintptr_t)(rb + (I * 3 + 1) * 1024);
    v += *(const LAS f32x4*)(uintptr_t)(rb + (I * 3 + 2) * 1024);
    constexpr int rt = I / 5, nt = I % 5;
    u32x2 w = {cvtpk(v[0], v[1]), cvtpk(v[2], v[3])};
    *(u32x2*)(outp + (size_t)(rt * 16) * (2 * DM) + nt * 16) = w;
  }
}
template <int W, int... Is> DI void op_finish(unsigned rb, uint16_t* outp, iseq<Is...>) {
  (op_red_write<W, Is>(rb), ...);
  asm volatile("s_waitcnt lgkmcnt(0)\n\ts_barrier" ::: "memory");
  (op_red_final<W, Is>(rb, outp), ...);
}

__global__ __launch_bounds__(256, 1)
void oproj_kernel(const uint16_t* __restrict__ Ows, const uint16_t* __restrict__ WOws, uint16_t* __restrict__ out) {
  extern __shared__ __attribute__((aligned(16))) char smem[];
  const int tid = threadIdx.x;
  const int wave = __builtin_amdgcn_readfirstlane(tid >> 6);
  const int l = tid & 63, c = l & 15, g = l >> 4;
  const int wg = blockIdx.x, xcd = wg & 7, wi = wg >> 3;
  const int blk = xcd >> 2, batch = (xcd & 3) * 4 + (wi >> 3), ng = wi & 7;
  Op o;
  o.rO = __builtin_amdgcn_make_buffer_rsrc((void*)(Ows + ((size_t)((blk * NB + batch) * 192 + wave * 48) * 9) * 512), 0, 48 * 9 * 1024, 0x00020000);
  o.rW = __builtin_amdgcn_make_buffer_rsrc((void*)(WOws + ((size_t)((blk * 8 + ng) * 192 + wave * 48) * 5) * 512), 0, 48 * 5 * 1024, 0x00020000);
#pragma unroll
  for (int i = 0; i < 9; ++i) { o.vo[i] = l * 16 + i * 1024; OPAQUE(o.vo[i]); }
  asm volatile("" ::: ACLOB, "a156","a157","a158","a159","a160","a161","a162","a163","a164","a165","a166","a167","a168","a169","a170","a171","a172","a173","a174","a175","a176","a177","a178","a179");
  op_load_all<0>(o, 0, make_iseq<14>{});
  op_load_all<1>(o, 1024, make_iseq<14>{});
  op_load_all<2>(o, 2048, make_iseq<14>{});
  GAP();
  op_step<0, true>(o, 3 * 1024);
  op_step<1, false>(o, 4 * 1024);
  op_step<2, false>(o, 5 * 1024);
  op_step<3, false>(o, 6 * 1024);
  for (int ks = 4; ks < 48; ks += 4) {
    const unsigned k3 = (ks + 3) * 1024, k4 = (ks + 4 < 48 ? ks + 4 : 47) * 1024, k5 = (ks + 5 < 48 ? ks + 5 : 47) * 1024, k6 = (ks + 6 < 48 ? ks + 6 : 47) * 1024;
    op_step<0, false>(o, k3);
    op_step<1, false>(o, k4);
    op_step<2, false>(o, k5);
    op_step<3, false>(o, k6);
  }
  asm volatile("s_nop 7" ::: "memory");
  GAP();
  const unsigned rb = (unsigned)(uintptr_t)(LAS char*)smem + l * 16;
  uint16_t* outp = out + (size_t)(batch * SQ + c) * (2 * DM) + blk * DM + ng * 80 + 4 * g;
  if (wave == 0)      op_finish<0>(rb, outp, make_iseq<45>{});
  else if (wave == 1) op_finish<1>(rb, outp, make_iseq<45>{});
  else if (wave == 2) op_finish<2>(rb, outp, make_iseq<45>{});
  else                op_finish<3>(rb, outp, make_iseq<45>{});
}

extern "C" void kernel_launch(void* const* d_in, const int* in_sizes, int n_in,
                              void* d_out, int out_size, void* d_ws, size_t ws_size,
                              hipStream_t stream) {
  (void)in_sizes; (void)n_in; (void)out_size; (void)ws_size;
  const uint16_t* xA = (const uint16_t*)d_in[0]; const uint16_t* kA = (const uint16_t*)d_in[1];
  const uint16_t* vA = (const uint16_t*)d_in[2]; const uint16_t* wqA = (const uint16_t*)d_in[3];
  const uint16_t* woA = (const uint16_t*)d_in[4];
  const uint16_t* xB = (const uint16_t*)d_in[5]; const uint16_t* kB = (const uint16_t*)d_in[6];
  const uint16_t* vB = (const uint16_t*)d_in[7]; const uint16_t* wqB = (const uint16_t*)d_in[8];
  const uint16_t* woB = (const uint16_t*)d_in[9];
  uint16_t* Ows = (uint16_t*)d_ws;
  uint16_t* WOws = (uint16_t*)((char*)d_ws + WS_WO_OFF);
  (void)hipFuncSetAttribute((const void*)attn_fused_kernel, hipFuncAttributeMaxDynamicSharedMemorySize, (int)LDS1_BYTES);
  (void)hipFuncSetAttribute((const void*)oproj_kernel, hipFuncAttributeMaxDynamicSharedMemorySize, (int)LDS2_BYTES);
  hipLaunchKernelGGL(attn_fused_kernel, dim3(256), dim3(256), LDS1_BYTES, stream,
                     xA, kA, vA, wqA, woA, xB, kB, vB, wqB, woB, Ows, WOws);
  hipLaunchKernelGGL(oproj_kernel, dim3(256), dim3(256), LDS2_BYTES, stream, Ows, WOws, (uint16_t*)d_out);
}
```

```cpp
#include <hip/hip_runtime.h>
#include <hip/hip_bf16.h>
#include <stdint.h>
#include <utility>

typedef __bf16   bf16x8 __attribute__((ext_vector_type(8)));
typedef __bf16   bf16x4 __attribute__((ext_vector_type(4)));
typedef __bf16   bf16x2 __attribute__((ext_vector_type(2)));
typedef float    f32x4  __attribute__((ext_vector_type(4)));
typedef float    f32x2  __attribute__((ext_vector_type(2)));
typedef unsigned u32x4  __attribute__((ext_vector_type(4)));
typedef unsigned u32x2  __attribute__((ext_vector_type(2)));
#define LAS __attribute__((address_space(3)))
#define DI __device__ __forceinline__
template <int... Is> using iseq = std::integer_sequence<int, Is...>;
template <int N> using make_iseq = std::make_integer_sequence<int, N>;

constexpr int NB = 16, SQ = 144, DM = 640, NH = 32, SK = 512, DH = 192;
constexpr int HD = NH * DH;
constexpr unsigned SLOT = 24576;
constexpr unsigned SCR_OFF = 6 * SLOT;
constexpr unsigned FLAG_OFF = SCR_OFF + 5120;
constexpr unsigned LDS1_BYTES = FLAG_OFF + 64;
constexpr float QSCALE = 0.07216878364870322f * 1.4426950408889634f;
constexpr float THR = 8.0f;
constexpr float THR_LO = 32.0f;

constexpr size_t WS_O_BYTES  = (size_t)2 * NB * SQ * HD * 2;
constexpr size_t WS_WO_OFF   = WS_O_BYTES;

#define KV_AUX 2
#define O_AUX 0
#define GAP() __builtin_amdgcn_sched_barrier(0)
#define OPAQUE(x) asm volatile("" : "+v"(x))

#define ACLOB "a0","a1","a2","a3","a4","a5","a6","a7","a8","a9","a10","a11","a12","a13","a14","a15","a16","a17","a18","a19","a20","a21","a22","a23","a24","a25","a26","a27","a28","a29","a30","a31","a32","a33","a34","a35","a36","a37","a38","a39","a40","a41","a42","a43","a44","a45","a46","a47","a48","a49","a50","a51","a52","a53","a54","a55","a56","a57","a58","a59","a60","a61","a62","a63","a64","a65","a66","a67","a68","a69","a70","a71","a72","a73","a74","a75","a76","a77","a78","a79","a80","a81","a82","a83","a84","a85","a86","a87","a88","a89","a90","a91","a92","a93","a94","a95","a96","a97","a98","a99","a100","a101","a102","a103","a104","a105","a106","a107","a108","a109","a110","a111","a112","a113","a114","a115","a116","a117","a118","a119","a120","a121","a122","a123","a124","a125","a126","a127","a128","a129","a130","a131","a132","a133","a134","a135","a136","a137","a138","a139","a140","a141","a142","a143","a144","a145","a146","a147","a148","a149","a150","a151","a152","a153","a154","a155"

#define ACLOB2 "a156","a157","a158","a159","a160","a161","a162","a163","a164","a165","a166","a167","a168","a169","a170","a171","a172","a173","a174","a175","a176","a177","a178","a179","a180","a181","a182","a183","a184","a185","a186","a187","a188","a189","a190","a191","a192","a193","a194","a195","a196","a197","a198","a199","a200","a201","a202","a203","a204","a205","a206","a207","a208","a209","a210","a211","a212","a213","a214","a215","a216","a217","a218","a219","a220","a221","a222","a223","a224","a225","a226","a227"

DI unsigned cvtpk(float lo, float hi) {
  f32x2 v = {lo, hi}; bf16x2 b = __builtin_convertvector(v, bf16x2); return __builtin_bit_cast(unsigned, b);
}
DI float max3f(float a, float b, float c) { float r; asm("v_max3_f32 %0, %1, %2, %3" : "=v"(r) : "v"(a), "v"(b), "v"(c)); return r; }
DI float min3f(float a, float b, float c) { float r; asm("v_min3_f32 %0, %1, %2, %3" : "=v"(r) : "v"(a), "v"(b), "v"(c)); return r; }
DI float max2f(float a, float b) { float r; asm("v_max_f32_e32 %0, %1, %2" : "=v"(r) : "v"(a), "v"(b)); return r; }
DI float fsub_s(float a, float b) { float r; asm("v_sub_f32_e32 %0, %1, %2" : "=v"(r) : "v"(a), "v"(b)); return r; }
DI bf16x8 lds_b128(unsigned addr) { return *(const LAS bf16x8*)(uintptr_t)addr; }
DI bf16x4 lds_tr(unsigned a) { return __builtin_amdgcn_ds_read_tr16_b64_v4bf16((LAS bf16x4*)(uintptr_t)a); }
DI bf16x8 cat4(bf16x4 lo, bf16x4 hi) { return __builtin_shufflevector(lo, hi, 0, 1, 2, 3, 4, 5, 6, 7); }
DI float xmax16_32(float v) {
  auto r = __builtin_amdgcn_permlane16_swap(__float_as_uint(v), __float_as_uint(v), false, false);
  v = fmaxf(__uint_as_float(r[0]), __uint_as_float(r[1]));
  auto s = __builtin_amdgcn_permlane32_swap(__float_as_uint(v), __float_as_uint(v), false, false);
  return fmaxf(__uint_as_float(s[0]), __uint_as_float(s[1]));
}

constexpr int AO(int rt, int t) { return 4 * (13 * rt + t); }
template <int R, bool Z> DI void mfma_acc(const bf16x8& a, const bf16x8& b) {
  if constexpr (Z) asm volatile("v_mfma_f32_16x16x32_bf16 a[%c2:%c3], %0, %1, 0" :: "v"(a), "v"(b), "i"(R), "i"(R + 3));
  else             asm volatile("v_mfma_f32_16x16x32_bf16 a[%c2:%c3], %0, %1, a[%c2:%c3]" :: "v"(a), "v"(b), "i"(R), "i"(R + 3));
}
template <int R> DI float acc_read() { float r; asm volatile("v_accvgpr_read_b32 %0, a[%c1]" : "=v"(r) : "i"(R)); return r; }
template <int R> DI void acc_scale4(float f) {
  float t0, t1;
  asm volatile("v_accvgpr_read_b32 %0, a[%c3]\n\tv_accvgpr_read_b32 %1, a[%c4]\n\tv_mul_f32 %0, %2, %0\n\tv_mul_f32 %1, %2, %1\n\t"
               "v_accvgpr_write_b32 a[%c3], %0\n\tv_accvgpr_write_b32 a[%c4], %1\n\t"
               "v_accvgpr_read_b32 %0, a[%c5]\n\tv_accvgpr_read_b32 %1, a[%c6]\n\tv_mul_f32 %0, %2, %0\n\tv_mul_f32 %1, %2, %1\n\t"
               "v_accvgpr_write_b32 a[%c5], %0\n\tv_accvgpr_write_b32 a[%c6], %1"
               : "=&v"(t0), "=&v"(t1) : "v"(f), "i"(R), "i"(R + 1), "i"(R + 2), "i"(R + 3));
}

struct St {
  unsigned aX0, aX1, aK0, aK1, aW[4], aV[4];
  unsigned bX0, bX1, bW[4], bK0, bK1, bV[4];
  f32x4 qa[3][12];
  bf16x8 qf[3][6];
  f32x4 S[2][3][2];
  u32x4 P[2][3];
  bf16x4 flo[6], fhi[6];
  bf16x8 fk[6];
  bf16x8 xf[2][3];
  f32x4 negm4[3];
  float mx[3], fct[3];
  bf16x8 ones;
  bool pend;
  unsigned aflag, adone, flagv, step, rs0, rs1;
  float inv[3], et[8];
  u32x4 ew;
  __amdgpu_buffer_rsrc_t rO;
  unsigned voO;
};

enum { PH_QP = 0, PH_QK = 1, PH_PV = 2 };
constexpr int PD = 5;

template <int PH, int HALF, int F, int PART> DI void read_frag(St& c) {
  if constexpr (PH == PH_QP) {
    constexpr int ks = F / 12, t = F % 12;
    const unsigned a = c.bW[t & 3] + (t >> 2) * 128 + ks * (32 * 384);
    if constexpr (PART == 0) c.flo[F % 6] = lds_tr(a); else c.fhi[F % 6] = lds_tr(a + 4 * 384);
  } else if constexpr (PH == PH_QK) {
    constexpr int kt = HALF * 2 + F / 6, s = F % 6;
    if constexpr (PART == 0) c.fk[F % 6] = lds_b128(((s & 1) ? c.bK1 : c.bK0) + kt * 6144 + (s >> 1) * 128);
  } else {
    const unsigned a = c.bV[F & 3] + (F >> 2) * 128 + HALF * (32 * 384);
    if constexpr (PART == 0) c.flo[F % 6] = lds_tr(a); else c.fhi[F % 6] = lds_tr(a + 16 * 384);
  }
}
template <int KS, int RT> DI void read_x(St& c) {
  c.xf[KS][RT] = lds_b128((KS ? c.bX1 : c.bX0) + RT * 2048); }
DI unsigned vadd_s(unsigned sv, unsigned v) { unsigned r; asm volatile("v_add_u32 %0, %1, %2" : "=v"(r) : "s"(sv), "v"(v)); return r; }
template <unsigned C> DI unsigned vadd_c(unsigned v) { unsigned r; asm volatile("v_add_u32 %0, %1, %2" : "=v"(r) : "i"(C), "v"(v)); return r; }
DI void flag_read(St& c) { c.flagv = *(volatile LAS unsigned*)(uintptr_t)c.aflag; }
DI void flag_wait(St& c, unsigned need) {
  if (__builtin_expect(__builtin_amdgcn_readfirstlane(c.flagv) < need, 0)) {
    unsigned tv, ts;
    asm volatile("1:\n\tds_read_b32 %0, %2\n\ts_waitcnt lgkmcnt(0)\n\tv_readfirstlane_b32 %1, %0\n\ts_cmp_lt_u32 %1, %3\n\ts_cbranch_scc1 1b"
                 : "=&v"(tv), "=&s"(ts) : "v"(c.aflag), "s"(need) : "memory", "scc");
  }
  asm volatile("" ::: "memory");
}
DI void done_signal(St& c) {
  asm volatile("" ::: "memory");
  c.step = vadd_c<1>(c.step);
  *(volatile LAS unsigned*)(uintptr_t)c.adone = c.step;
}

constexpr int SM_NOPS = 13 + 36;
template <int X, bool FIRST, int I> DI void sm_op(St& c) {
  if constexpr (I < 12) {
    constexpr int kt2 = I / 6, rt = (I % 6) % 3, st = (I % 6) / 3;
    const f32x4& s = c.S[X][rt][kt2];
    if constexpr (kt2 == 0 && st == 0) c.mx[rt] = max3f(s[0], s[1], s[2]);
    else if constexpr (kt2 == 0)       c.mx[rt] = max2f(c.mx[rt], s[3]);
    else if constexpr (st == 0)        c.mx[rt] = max3f(c.mx[rt], s[0], s[1]);
    else                               c.mx[rt] = max3f(c.mx[rt], s[2], s[3]);
  } else if constexpr (I == 12) {
    bool bad = max3f(c.mx[0], c.mx[1], c.mx[2]) > THR;
    if constexpr (FIRST) bad = bad || (min3f(c.mx[0], c.mx[1], c.mx[2]) < -THR_LO);
    if (__builtin_expect(__any((int)bad), 0)) {
#pragma unroll
      for (int rt = 0; rt < 3; ++rt) {
        float d = xmax16_32(c.mx[rt]);
        if constexpr (!FIRST) d = fmaxf(d, 0.f);
        const float nm = c.negm4[rt][0] - d;
        c.negm4[rt] = f32x4{nm, nm, nm, nm}; OPAQUE(c.negm4[rt]);
#pragma unroll
        for (int kt2 = 0; kt2 < 2; ++kt2)
#pragma unroll
          for (int r = 0; r < 4; ++r) c.S[X][rt][kt2][r] = fsub_s(c.S[X][rt][kt2][r], d);
        c.fct[rt] = __builtin_amdgcn_exp2f(-d);
      }
      if constexpr (!FIRST) c.pend = true;
    }
  } else {
    constexpr int J = I - 13, rt = J / 12, k = J % 12;
    if constexpr (k < 8) {
      float e = __builtin_amdgcn_exp2f(c.S[X][rt][k >> 2][k & 3]); OPAQUE(e);
      c.S[X][rt][k >> 2][k & 3] = e;
    } else {
      constexpr int p = k - 8;
      unsigned w = cvtpk(c.S[X][rt][p >> 1][2 * (p & 1)], c.S[X][rt][p >> 1][2 * (p & 1) + 1]); OPAQUE(w);
      c.P[X][rt][p] = w;
    }
  }
}
template <int X, bool FIRST, int LO, int... Is> DI void sm_ops_(St& c, iseq<Is...>) { (sm_op<X, FIRST, LO + Is>(c), ...); }
template <int X, bool FIRST, int LO, int HI> DI void sm_ops(St& c) { if constexpr (HI > LO) sm_ops_<X, FIRST, LO>(c, make_iseq<HI - LO>{}); }

constexpr int sm_cost(int i) { return i < 12 ? 4 : (i == 12 ? 8 : (((i - 13) % 12) < 8 ? 8 : 4)); }
struct SmSched { int op[75][2]; int last; int pv_done[SM_NOPS]; };
constexpr SmSched make_sched() {
  SmSched s{};
  int at[SM_NOPS] = {};
  for (int i = 0; i < SM_NOPS; ++i) at[i] = -1;
  s.last = 0;
  for (int gi = 0; gi < 75; ++gi) {
    s.op[gi][0] = -1; s.op[gi][1] = -1;
    const bool pv = gi < 39;
    const int m = pv ? gi : gi - 39, grp = m / 3, r = m % 3;
    int cap = 8;
    if (pv) { if (grp < 12 - PD) { if (r < 2) cap = 4; } else if (grp < 12) { if (r == 0) cap = 4; } }
    else {
      if (grp < 12 - PD) { if (r == 0) cap = 4; } else { if (r < 2) cap = 4; }
      if (grp < 4 && r == 2) cap = 4;
      if (grp == 3 && r == 1) cap = 4;
      if (grp == 6 && r >= 1) cap = 0;
      if ((grp == 7 || grp == 8) && r == 2) cap = 4;
    }
    int n = 0;
    for (int pass = 0; pass < 2; ++pass)
      for (int i = 0; i < SM_NOPS && n < 2; ++i) {
        if (at[i] >= 0) continue;
        const int cst = sm_cost(i);
        if ((pass == 0) != (cst == 8) || cst > cap) continue;
        bool ok = true;
        if (i < 12) { if (i >= 3) ok = at[i - 3] >= 0 && at[i - 3] < gi; if (i >= 6 && gi < 4) ok = false; }
        else if (i == 12) { for (int k = 0; k < 12; ++k) ok = ok && at[k] >= 0 && at[k] < gi; }
        else {
          const int J = i - 13, rt = J / 12, k = J % 12;
          if (k < 8) ok = at[12] >= 0 && at[12] < gi;
          else { const int e0 = 13 + rt * 12 + 2 * (k - 8); ok = at[e0] >= 0 && at[e0] < gi && at[e0 + 1] >= 0 && at[e0 + 1] < gi; }
        }
        if (!ok) continue;
        at[i] = gi; s.op[gi][n++] = i; cap -= cst; s.last = gi;
      }
  }
  for (int i = 0; i < SM_NOPS; ++i) { s.pv_done[i] = (at[i] >= 0 && at[i] < 39) ? 1 : 0; if (at[i] < 0) s.last = 1000; }
  return s;
}
constexpr SmSched SCHED_N = make_sched();
static_assert(SCHED_N.last <= 72, "softmax schedule must finish before the last gaps of the QK phase");
static_assert(SCHED_N.pv_done[12] == 1, "the max check must sit inside the PV phase (the rescale follows that phase)");
template <int X, int... Is> DI void sm_rest_(St& c, iseq<Is...>) { ((SCHED_N.pv_done[Is] ? (void)0 : sm_op<X, false, Is>(c)), ...); }

template <int... Is> DI void rescale_(St& c, iseq<Is...>) { (acc_scale4<4 * Is>(c.fct[Is / 13]), ...); }
DI void rescale(St& c) {
  asm volatile("s_nop 7" ::: "memory");
  rescale_(c, make_iseq<39>{});
  asm volatile("s_nop 1" ::: "memory");
}

constexpr int EP_NOPS = 6 + 18 * 24;
constexpr int ASTASH = 156;
template <bool DIRECT, int I0> DI void ep_op(St& c) {
  if constexpr (I0 < 6) {
    constexpr int rt = I0 % 3;
    if constexpr (I0 < 3) c.inv[rt] = acc_read<AO(rt, 12)>();
    else { c.inv[rt] = __builtin_amdgcn_rcpf(c.inv[rt]); OPAQUE(c.inv[rt]); }
  } else {
    constexpr int I = I0 - 6, q = I / 24, k = I % 24, ES = q / 3, rt = q % 3;
    if constexpr (k < 8) c.et[k] = acc_read<AO(rt, 2 * ES + (k >> 2)) + (k & 3)>();
    else if constexpr (k < 16) { c.et[k - 8] = c.et[k - 8] * c.inv[rt]; OPAQUE(c.et[k - 8]); }
    else if constexpr (k < 20) { unsigned w = cvtpk(c.et[2 * (k - 16)], c.et[2 * (k - 16) + 1]); OPAQUE(w); c.ew[k - 16] = w; }
    else if constexpr (DIRECT) { if constexpr (k == 20) __builtin_amdgcn_raw_buffer_store_b128(c.ew, c.rO, c.voO, (ES * 9 + rt) * 1024, O_AUX); }
    else asm volatile("v_accvgpr_write_b32 a[%c1], %0" :: "v"(c.ew[k - 20]), "i"(ASTASH + 4 * q + (k - 20)));
  }
}
template <bool DIRECT, int LO, int... Is> DI void ep_ops_(St& c, iseq<Is...>) { (ep_op<DIRECT, LO + Is>(c), ...); }
template <bool DIRECT, int LO, int HI> DI void ep_ops(St& c) {
  constexpr int H2 = HI > EP_NOPS ? EP_NOPS : HI;
  if constexpr (H2 > LO) ep_ops_<DIRECT, LO>(c, make_iseq<H2 - LO>{});
}
template <int Q> DI void stash_store(St& c) {
  constexpr int ES = Q / 3, rt = Q % 3;
  asm volatile("buffer_store_dwordx4 a[%c3:%c4], %0, %1, %2 offen" :: "v"(c.voO), "s"(c.rO), "s"((unsigned)((ES * 9 + rt) * 1024)), "i"(ASTASH + 4 * Q), "i"(ASTASH + 4 * Q + 3) : "memory");
}

constexpr int ep_slot(int M) {
  int n = 0;
  for (int m = 0; m < M; ++m) {
    const int g = m / 3, r = m % 3;
    int cap = (r < 2) ? 1 : 2;
    if (r == 2 && g >= 4 && g < 7) cap = 1;
    if (r == 2 && g == 10) cap = 0;
    if (r == 2 && g == 13) cap = 1;
    if (r == 2 && g >= 15 && g <= 20) cap = 0;
    if (r == 2 && g == 21) cap = 1;
    if (r == 2 && g == 23) cap = 1;
    n += cap;
  }
  return n;
}
constexpr int EP_PER = ep_slot(72);
static_assert(EP_PER * 7 >= EP_NOPS, "epilogue ops must fit in 7 Q-projection steps");
template <int PH, int HALF, int X, int NPH, int NHALF, int NL0, int NL1, int ND, bool Z, int SM, int ES, int SQ_, int M>
DI void gap(St& c, unsigned nb) {
  constexpr bool SYNC = NL0 != -1, RTS = NL0 == -2;
  constexpr int NGF = (PH == PH_QP) ? 24 : 12;
  constexpr int g = M / 3, rt = M % 3;
  constexpr unsigned S0 = NL0 >= 0 ? (unsigned)(NL0 % 6) * SLOT : 0u, S1 = NL1 >= 0 ? (unsigned)(NL1 % 6) * SLOT : 0u;
#define ADD0(v) (RTS ? vadd_s(c.rs0, v) : vadd_c<S0>(v))
#define ADD1(v) (RTS ? vadd_s(c.rs1, v) : vadd_c<S1>(v))
  if constexpr (PH == PH_QP) {
    constexpr int ks = g / 12, t = g % 12;
    const bf16x8 f = cat4(c.flo[g % 6], c.fhi[g % 6]);
    if constexpr (Z && ks == 0) asm volatile("v_mfma_f32_16x16x32_bf16 %0, %1, %2, 0" : "=&v"(c.qa[rt][t]) : "v"(f), "v"(c.xf[ks][rt]));
    else                        asm volatile("v_mfma_f32_16x16x32_bf16 %0, %1, %2, %0" : "+v"(c.qa[rt][t]) : "v"(f), "v"(c.xf[ks][rt]));
  } else if constexpr (PH == PH_QK) {
    constexpr int kt2 = g / 6, s = g % 6;
    if constexpr (s == 0) asm volatile("v_mfma_f32_16x16x32_bf16 %0, %1, %2, %3" : "=&v"(c.S[X][rt][kt2]) : "v"(c.fk[g % 6]), "v"(c.qf[rt][s]), "v"(c.negm4[rt]));
    else                  asm volatile("v_mfma_f32_16x16x32_bf16 %0, %1, %2, %0" : "+v"(c.S[X][rt][kt2]) : "v"(c.fk[g % 6]), "v"(c.qf[rt][s]));
  } else {
    const bf16x8 pf = __builtin_bit_cast(bf16x8, c.P[X][rt]);
    if constexpr (g < 12) mfma_acc<AO(rt, g), Z>(cat4(c.flo[g % 6], c.fhi[g % 6]), pf);
    else                  mfma_acc<AO(rt, 12), Z>(c.ones, pf);
  }
  GAP();
  if constexpr (PH == PH_QK && M == 24) asm volatile("" :: "v"(c.negm4[0]), "v"(c.negm4[1]), "v"(c.negm4[2]));
  if constexpr (rt < 2) {
    if constexpr (g + PD < NGF) read_frag<PH, HALF, g + PD, rt>(c);
    else if constexpr (g + PD - NGF < PD) read_frag<NPH, NHALF, g + PD - NGF, rt>(c);
  }
  if constexpr (PH == PH_QP && g >= 4 && g < 7 && rt == 2) read_x<1, g - 4>(c);
  if constexpr (NPH == PH_QP && g == NGF - 5 && rt == 2) { read_x<0, 0>(c); read_x<0, 1>(c); }
  if constexpr (NPH == PH_QP && g == NGF - 4 && rt == 2) read_x<0, 2>(c);
  if constexpr (SYNC) {
    if constexpr (PH == PH_QK) {
      if constexpr (g == 3 && rt == 1) flag_read(c);
      if constexpr (g == 6 && rt == 1) flag_wait(c, nb + ND);
      if constexpr (g == 6 && rt == 2) done_signal(c);
      if constexpr (g < 4 && rt == 2) c.bV[g] = ADD1(c.aV[g]);
      if constexpr (g == 7 && rt == 2) c.bK0 = ADD0(c.aK0);
      if constexpr (g == 8 && rt == 2) c.bK1 = ADD0(c.aK1);
    } else if constexpr (PH == PH_QP) {
      if constexpr (g == 13 && rt == 2) flag_read(c);
      if constexpr (g == 17 && rt == 2) flag_wait(c, nb + ND);
      if constexpr (g == 18 && rt == 2) done_signal(c);
      if constexpr (NPH == PH_QK) {
        if constexpr (g == 10 && rt == 2) { c.bK0 = ADD0(c.aK0); c.bK1 = ADD0(c.aK1); }
      } else {
        if constexpr (g == 10 && rt == 2) { c.bX0 = ADD0(c.aX0); c.bX1 = ADD0(c.aX1); }
        if constexpr (g == 15 && rt == 2) c.bW[0] = ADD1(c.aW[0]);
        if constexpr (g == 16 && rt == 2) c.bW[1] = ADD1(c.aW[1]);
        if constexpr (g == 20 && rt == 2) c.bW[2] = ADD1(c.aW[2]);
        if constexpr (g == 21 && rt == 2) c.bW[3] = ADD1(c.aW[3]);
      }
    } else {
      if constexpr (g == 3 && rt == 2) flag_read(c);
      if constexpr (g == 5 && rt == 2) flag_wait(c, nb + ND);
      if constexpr (g == 6 && rt == 2) done_signal(c);
      if constexpr (g == 0 && rt == 2) { c.bX0 = ADD0(c.aX0); c.bX1 = ADD0(c.aX1); }
      if constexpr (g == 1 && rt == 2) { c.bW[0] = ADD1(c.aW[0]); c.bW[1] = ADD1(c.aW[1]); }
      if constexpr (g == 2 && rt == 2) { c.bW[2] = ADD1(c.aW[2]); c.bW[3] = ADD1(c.aW[3]); }
    }
  }
  if constexpr (SM == 1) {
    constexpr int gi = (PH == PH_PV) ? M : 39 + M;
    if constexpr (SCHED_N.op[gi][0] >= 0) sm_op<X ^ 1, false, SCHED_N.op[gi][0]>(c);
    if constexpr (SCHED_N.op[gi][1] >= 0) sm_op<X ^ 1, false, SCHED_N.op[gi][1]>(c);
  } else if constexpr (SM == 2) {
    if constexpr (M >= 24 && M < 30) sm_ops<X, true, M - 24, M - 23>(c);
  } else if constexpr (SM == 3) {
    if constexpr (M < 34) sm_ops<X ^ 1, true, 13 + M, 14 + M>(c);
    if constexpr (M == 33) sm_ops<X ^ 1, true, 13 + 34, 13 + 36>(c);
  }
  if constexpr (ES >= 0) {
    constexpr int e0 = ES * EP_PER + ep_slot(M), e1 = ES * EP_PER + ep_slot(M + 1);
    ep_ops<false, e0, e1>(c);
  }
  if constexpr (SQ_ >= 0 && M == (PH == PH_QP ? 71 : (PH == PH_QK ? 35 : 38))) stash_store<SQ_>(c);
  GAP();
}
template <int PH, int HALF, int X, int NPH, int NHALF, int NL0, int NL1, int ND, bool Z, int SM, int ES, int SQ_, int... Ms>
DI void phase_(St& c, unsigned nb, iseq<Ms...>) { (gap<PH, HALF, X, NPH, NHALF, NL0, NL1, ND, Z, SM, ES, SQ_, Ms>(c, nb), ...); }
template <int PH, int HALF, int X, int NPH, int NHALF, int NL0, int NL1, int ND, bool Z, int SM, int ES = -1, int SQ_ = -1>
DI void phase(St& c, unsigned nb) {
  constexpr int NM = (PH == PH_QP) ? 72 : (PH == PH_QK ? 36 : 39);
  phase_<PH, HALF, X, NPH, NHALF, NL0, NL1, ND, Z, SM, ES, SQ_>(c, nb, make_iseq<NM>{});
  if constexpr (PH == PH_PV && SM == 1) {
    if (__builtin_expect(c.pend, 0)) { rescale(c); c.pend = false; }
    GAP();
  }
}

template <int RT, int S> DI void q_convert(St& c) {
  const f32x4 a = c.qa[RT][2 * S] * QSCALE, b = c.qa[RT][2 * S + 1] * QSCALE;
  unsigned pa0 = cvtpk(a[0], a[1]), pa1 = cvtpk(a[2], a[3]);
  unsigned pb0 = cvtpk(b[0], b[1]), pb1 = cvtpk(b[2], b[3]);
  auto r0 = __builtin_amdgcn_permlane32_swap(pa0, pb0, false, false);
  auto r1 = __builtin_amdgcn_permlane32_swap(pa1, pb1, false, false);
  auto t0 = __builtin_amdgcn_permlane16_swap(r0[0], r0[1], false, false);
  auto t1 = __builtin_amdgcn_permlane16_swap(r1[0], r1[1], false, false);
  u32x4 w = {t0[0], t1[0], t0[1], t1[1]};
  c.qf[RT][S] = __builtin_bit_cast(bf16x8, w);
}
template <int... Is> DI void q_convert_all(St& c, iseq<Is...>) { (q_convert<Is % 3, Is / 3>(c), ...); }

__host__ __device__ constexpr unsigned slot_of(int n) { return (unsigned)(n % 6) * SLOT; }
__host__ __device__ constexpr int kload(int j) { return j == 0 ? 20 : 2 * j + 19; }
__host__ __device__ constexpr int vload(int j) { return j == 7 ? 35 : 2 * j + 22; }

struct Ld {
  __amdgpu_buffer_rsrc_t rX, rK, rV, rW;
  unsigned vK[6], vV[3], vW[12], vX[4], vL[3];
  unsigned ldsb, sW[2], sX[2], sK[2];
  u32x4 buf[2][24];
  __amdgpu_buffer_rsrc_t rWO, rWF;
  unsigned sWO, sWF, vWO, vWA, vWR, vWF;
  unsigned base_step, base_load;
};
#define DMA16I(rsrc, ldsoff, voff, soff, imm, aux) \
  __builtin_amdgcn_raw_ptr_buffer_load_lds((rsrc), (LAS void*)(uintptr_t)(ldsoff), 16, (voff), (soff), (imm), (aux))
constexpr int c_hi(int T) { return T < 0 ? (T == -1 ? 0 : -1) : (T < 10 ? 2 * T + 2 : (T == 10 ? 21 : (T < 18 ? 2 * T + 1 : (T == 18 ? 36 : 38)))); }
#define NFIFO 8
static_assert(NFIFO <= 8, "FIFO entry 2 is reused by the wo re-layout from step 13 on");
constexpr int xw_instr(int N) { return ((N % 36) & 1) == 0 ? 18 : 24; }
struct LSeq { int id[96]; int cnt[96]; int step[96]; int n; };
constexpr int kv_chunks_at(int T, int k) {
  if (T == 17) return k < 2 ? k : -1;
  if (T == 18) return k < 2 ? 2 + k : -1;
  if (T < 8) return k < 1 ? 4 + T : -1;
  return -1;
}
constexpr LSeq make_lseq() {
  LSeq q{}; q.n = 0;
  for (int T = 0; T < 19; ++T) {
    const int lo = c_hi(T - 2) + 6, hi = c_hi(T - 1) + 6;
    for (int n = lo; n < hi; ++n) {
      if ((n % 36) < 20) { q.id[q.n] = n; q.cnt[q.n] = xw_instr(n); q.step[q.n] = T; ++q.n; }
      else if (n - 20 >= NFIFO) { q.id[q.n] = n; q.cnt[q.n] = 24; q.step[q.n] = T; ++q.n; }
      else { const int i = n - 20; if (i + 4 < NFIFO) for (int cch = 0; cch < 3; ++cch) { q.id[q.n] = 100 + 3 * (i + 4) + cch; q.cnt[q.n] = 8; q.step[q.n] = T; ++q.n; } }
    }
    if (T == 13) { q.id[q.n] = 300; q.cnt[q.n] = 20; q.step[q.n] = T; ++q.n; }
    if (T == 15) { q.id[q.n] = 301; q.cnt[q.n] = 20; q.step[q.n] = T; ++q.n; }
    for (int k = 0; k < 2; ++k) {
      const int ch = kv_chunks_at(T, k);
      if (ch >= 0) { q.id[q.n] = 100 + (T >= 17 ? 48 : 0) + ch; q.cnt[q.n] = 8; q.step[q.n] = T; ++q.n; }
    }
  }
  return q;
}
constexpr LSeq LSEQ = make_lseq();
constexpr int lseq_pos(int id) { for (int k = 0; k < LSEQ.n; ++k) if (LSEQ.id[k] == id) return k; return -1; }
constexpr int younger(int id, int cur) {
  int p = lseq_pos(id);
  int r = 0;
  if (p >= 0 && p < cur) { for (int k = p + 1; k < cur; ++k) r += LSEQ.cnt[k]; return r; }
  const int pid = id < 100 ? id + 36 : id + 48;
  p = lseq_pos(pid);
  for (int k = p + 1; k < LSEQ.n; ++k) r += LSEQ.cnt[k];
  for (int k = 0; k < cur; ++k) r += LSEQ.cnt[k];
  return r;
}
template <int N> DI void vm_wait() { asm volatile("s_waitcnt vmcnt(%0)" :: "i"(N > 63 ? 63 : N) : "memory"); }

template <int N, int P> DI void xw_piece(Ld& ld) {
  constexpr int n = N % 36, nx = N / 36;
  const unsigned l0 = ld.ldsb + slot_of(n) + (P >> 2) * 4096;
  constexpr int imm = (P & 3) * 1024;
  if constexpr ((n & 1) == 0) DMA16I(ld.rX, l0, ld.vX[P & 3], ld.sX[nx] + (n >> 1) * 128 + (P >> 2) * (32 * DM * 2), imm, 0);
  else                        DMA16I(ld.rW, l0, ld.vW[P % 12], ld.sW[nx] + (unsigned)((n >> 1) * 64 + (P / 12) * 32) * (HD * 2), imm, 0);
}
template <int N, int... Ps> DI void xw_load_(Ld& ld, iseq<Ps...>) { (xw_piece<N, Ps>(ld), ...); }
template <int N> DI void xw_load(Ld& ld) {
  xw_load_<N>(ld, make_iseq<xw_instr(N)>{}); }

constexpr bool kv_isK(int i) { return i == 0 || (i < 15 && (i & 1)); }
constexpr int kv_j(int i) { return kv_isK(i) ? (i + 1) >> 1 : (i == 15 ? 7 : (i - 2) >> 1); }
template <int I, int P> DI void kv_issue_piece(Ld& ld) {
  constexpr int i = I % 16, nx = I / 16, e = i & 3, R = 96 * e + 4 * P;
  const unsigned so = ld.sK[nx] + kv_j(i) * SLOT + (P >> 2) * 4096;
  const unsigned vo = kv_isK(i) ? ld.vK[P % 6] : ld.vV[P % 3];
  const __amdgpu_buffer_rsrc_t rs = kv_isK(i) ? ld.rK : ld.rV;
  if constexpr (e < 2) asm volatile("buffer_load_dwordx4 a[%c3:%c4], %0, %1, %2 offen offset:%c5 nt" :: "v"(vo), "s"(rs), "s"(so), "i"(R), "i"(R + 3), "i"((P & 3) * 1024) : "memory");
  else                 asm volatile("buffer_load_dwordx4 %0, %1, %2, %3 offen offset:%c4 nt" : "=v"(ld.buf[e - 2][P]) : "v"(vo), "s"(rs), "s"(so), "i"((P & 3) * 1024) : "memory");
}
template <int I, int P0, int... Ps> DI void kv_issue_(Ld& ld, iseq<Ps...>) { (kv_issue_piece<I, P0 + Ps>(ld), ...); }
template <int I, int CH> DI void kv_chunk(Ld& ld) { kv_issue_<I, 8 * CH>(ld, make_iseq<8>{}); }
template <int I, int P> DI void kv_write_piece(Ld& ld) {
  constexpr int e = I & 3, R = 96 * e + 4 * P, sl = (20 + I) % 6;
  constexpr int off = (sl & 1) * SLOT + P * 1024;
  if constexpr (e < 2) asm volatile("ds_write_b128 %0, a[%c1:%c2] offset:%c3" :: "v"(ld.vL[sl >> 1]), "i"(R), "i"(R + 3), "i"(off) : "memory");
  else                 asm volatile("ds_write_b128 %0, %1 offset:%c2" :: "v"(ld.vL[sl >> 1]), "v"(ld.buf[e - 2][P]), "i"(off) : "memory");
}
template <int I, int... Ps> DI void kv_write_(Ld& ld, iseq<Ps...>) { (kv_write_piece<I, Ps>(ld), ...); }

DI void publish(Ld& ld, unsigned v) {
  asm volatile("ds_write_b32 %0, %1" :: "v"(ld.ldsb + FLAG_OFF), "v"(v) : "memory");
}
DI void wait_done(Ld& ld, unsigned need) {
  while (true) {
    u32x4 d;
    asm volatile("ds_read_b128 %0, %1\n\ts_waitcnt lgkmcnt(0)" : "=v"(d) : "v"(ld.ldsb + FLAG_OFF + 16) : "memory");
    const unsigned m = __builtin_amdgcn_readfirstlane(min(min(d[0], d[1]), d[2]));
    if (m >= need) break;
    __builtin_amdgcn_s_sleep(1);
  }
}
constexpr int lseq_before(int T, int n) {
  int k = 0;
  while (k < LSEQ.n && LSEQ.step[k] < T) ++k;
  const int lo = c_hi(T - 2) + 6, hi = c_hi(T - 1) + 6;
  for (int m = lo; m < hi && m < n; ++m) { if ((m % 36) < 20 || m - 20 >= NFIFO) k += 1; else if (m - 20 + 4 < NFIFO) k += 3; }
  return k;
}
template <int I, int P> DI void kv_dma_piece(Ld& ld) {
  const unsigned l0 = ld.ldsb + slot_of(20 + I) + (P >> 2) * 4096;
  constexpr int imm = (P & 3) * 1024;
  if constexpr (kv_isK(I)) DMA16I(ld.rK, l0, ld.vK[P % 6], ld.sK[0] + kv_j(I) * SLOT + (P >> 2) * 4096, imm, KV_AUX);
  else                     DMA16I(ld.rV, l0, ld.vV[P % 3], ld.sK[0] + kv_j(I) * SLOT + (P >> 2) * 4096, imm, KV_AUX);
}
template <int I, int... Ps> DI void kv_dma_(Ld& ld, iseq<Ps...>) { (kv_dma_piece<I, Ps>(ld), ...); }
template <int T, int N> DI void loader_load(Ld& ld) {
  if constexpr ((N % 36) < 20) xw_load<N>(ld);
  else if constexpr (N - 20 >= NFIFO) {
    kv_dma_<N - 20>(ld, make_iseq<24>{});
  } else {
    constexpr int i = N - 20;
    vm_wait<younger(100 + 3 * i + 2, lseq_before(T, N))>();
    kv_write_<i>(ld, make_iseq<24>{});
    asm volatile("s_waitcnt lgkmcnt(0)" ::: "memory");
    publish(ld, ld.base_load + N + 1);
    if constexpr (i + 4 < NFIFO) { kv_chunk<i + 4, 0>(ld); kv_chunk<i + 4, 1>(ld); kv_chunk<i + 4, 2>(ld); }
  }
}
template <int I> DI void wo_load(Ld& ld) {
  asm volatile("buffer_load_dwordx4 %0, %1, %2, %3 offen offset:%c4" : "=v"(ld.buf[0][I]) : "v"(ld.vWO), "s"(ld.rWO), "s"(ld.sWO + (I & 3) * (8 * DM * 2)), "i"((I >> 2) * 128) : "memory");
}
template <int... Is> DI void wo_loads(Ld& ld, iseq<Is...>) { (wo_load<Is>(ld), ...); }
template <int CH> DI void wo_chunk(Ld& ld) {
  u32x2 a0, a1, a2, a3, b0, b1, b2, b3;
  asm volatile("ds_write_b128 %8, %10\n\tds_write_b128 %8, %11 offset:1280\n\tds_write_b128 %8, %12 offset:2560\n\tds_write_b128 %8, %13 offset:3840\n\t"
               "ds_read_b64_tr_b16 %0, %9\n\tds_read_b64_tr_b16 %4, %9 offset:2560\n\t"
               "ds_read_b64_tr_b16 %1, %9 offset:32\n\tds_read_b64_tr_b16 %5, %9 offset:2592\n\t"
               "ds_read_b64_tr_b16 %2, %9 offset:64\n\tds_read_b64_tr_b16 %6, %9 offset:2624\n\t"
               "ds_read_b64_tr_b16 %3, %9 offset:96\n\tds_read_b64_tr_b16 %7, %9 offset:2656\n\t"
               "s_waitcnt lgkmcnt(0)"
               : "=&v"(a0), "=&v"(a1), "=&v"(a2), "=&v"(a3), "=&v"(b0), "=&v"(b1), "=&v"(b2), "=&v"(b3)
               : "v"(ld.vWA), "v"(ld.vWR), "v"(ld.buf[0][CH * 4]), "v"(ld.buf[0][CH * 4 + 1]), "v"(ld.buf[0][CH * 4 + 2]), "v"(ld.buf[0][CH * 4 + 3]) : "memory");
#define WO_ST(t, lo, hi) { constexpr int nt = CH * 4 + t; u32x4 w = {lo[0], lo[1], hi[0], hi[1]}; \
    __builtin_amdgcn_raw_buffer_store_b128(w, ld.rWF, ld.vWF, ld.sWF + ((nt / 5) * 192 * 5 + nt % 5) * 1024, 0); }
  WO_ST(0, a0, b0) WO_ST(1, a1, b1) WO_ST(2, a2, b2) WO_ST(3, a3, b3)
#undef WO_ST
}
template <int T, int LO, int... Is> DI void loader_loads_(Ld& ld, iseq<Is...>) { (loader_load<T, LO + Is>(ld), ...); }
template <int T> DI void loader_step(Ld& ld) {
  constexpr int lo = c_hi(T - 2) + 6, hi = c_hi(T - 1) + 6, need = c_hi(T + 1);
  asm volatile("" : "+s"(ld.ldsb), "+s"(ld.sW[0]), "+s"(ld.sX[0]), "+s"(ld.sK[0]));
  wait_done(ld, ld.base_step + T);
  if constexpr (T == 8) {
    vm_wait<younger(19, lseq_before(T, 0))>();
    publish(ld, ld.base_load + 20);
  }
  loader_loads_<T, lo>(ld, make_iseq<hi - lo>{});
  if constexpr (T != 8 && (need <= 20 || need - 1 - 20 >= NFIFO)) {
    vm_wait<younger(need - 1, lseq_before(T, 1000))>();
    publish(ld, ld.base_load + need);
  }
  if constexpr (T == 13) wo_loads(ld, make_iseq<20>{});
  if constexpr (T == 15) {
    vm_wait<younger(300, lseq_before(T, 1000))>();
    wo_chunk<0>(ld); wo_chunk<1>(ld); wo_chunk<2>(ld); wo_chunk<3>(ld); wo_chunk<4>(ld);
    asm volatile("" ::: "memory");
  }
  if constexpr (kv_chunks_at(T, 0) >= 0) { constexpr int ch = kv_chunks_at(T, 0); kv_chunk<(T >= 17 ? 16 : 0) + ch / 3, ch % 3>(ld); }
  if constexpr (kv_chunks_at(T, 1) >= 0) { constexpr int ch = kv_chunks_at(T, 1); kv_chunk<(T >= 17 ? 16 : 0) + ch / 3, ch % 3>(ld); }
}
template <int... Ts> DI void loader_unit(Ld& ld, iseq<Ts...>) { (loader_step<Ts>(ld), ...); }

__global__ __launch_bounds__(256, 1)
void attn_fused_kernel(const uint16_t* __restrict__ xA, const uint16_t* __restrict__ kA, const uint16_t* __restrict__ vA,
                       const uint16_t* __restrict__ wqA, const uint16_t* __restrict__ woA,
                       const uint16_t* __restrict__ xB, const uint16_t* __restrict__ kB, const uint16_t* __restrict__ vB,
                       const uint16_t* __restrict__ wqB, const uint16_t* __restrict__ woB,
                       uint16_t* __restrict__ Ows, uint16_t* __restrict__ WOws) {
  extern __shared__ __attribute__((aligned(16))) char smem[];
  const unsigned ldsb = (unsigned)(uintptr_t)(LAS char*)smem;
  const int tid = threadIdx.x;
  const int wave = __builtin_amdgcn_readfirstlane(tid >> 6);
  const int l = tid & 63;
  const int wg = blockIdx.x;
  const int xcd = wg & 7, wi = wg >> 3;
  const int blk = xcd >> 2;
  const int head = (xcd & 3) * 8 + (wi & 7);
  const int bsub = wi >> 3;

  if (tid < 16) *(volatile LAS unsigned*)(uintptr_t)(ldsb + FLAG_OFF + tid * 4) = 0u;
  asm volatile("s_waitcnt lgkmcnt(0)\n\ts_barrier" ::: "memory");

  if (wave == 3) {
    const uint16_t* xp = blk ? xB : xA; const uint16_t* kp = blk ? kB : kA;
    const uint16_t* vp = blk ? vB : vA; const uint16_t* wqp = blk ? wqB : wqA;
    Ld ld;
    asm volatile("" ::: "a191");
    ld.ldsb = ldsb;
    const unsigned l3 = l >> 3, l7 = l & 7;
#pragma unroll
    for (int p = 0; p < 12; ++p) {
      const unsigned line = 8 * p + l3, row = line / 3, seg = line % 3;
      const unsigned f = (((row >> 1) & 1) | ((row >> 2) & 2)) << 1;
      ld.vW[p] = row * (HD * 2) + seg * 128 + ((l7 ^ f) << 4) - (p & 3) * 1024;
      if (p < 6) ld.vK[p] = l3 * 128 + ((l7 ^ ((row >> 1) & 7)) << 4);
      if (p < 3) ld.vV[p] = l3 * 128 + ((l7 ^ (((row >> 1) & 3) << 1)) << 4);
      if (p < 4) ld.vX[p] = line * (DM * 2) + ((l7 ^ ((line >> 1) & 7)) << 4) - p * 1024;
      if (p < 3) ld.vL[p] = ldsb + p * 2 * SLOT + l * 16;
    }
    ld.sW[0] = ld.sW[1] = head * (DH * 2);
    ld.rX = __builtin_amdgcn_make_buffer_rsrc((void*)xp, 0, NB * SQ * DM * 2, 0x00020000);
    ld.rK = __builtin_amdgcn_make_buffer_rsrc((void*)kp, 0, NB * NH * SK * DH * 2, 0x00020000);
    ld.rV = __builtin_amdgcn_make_buffer_rsrc((void*)vp, 0, NB * NH * SK * DH * 2, 0x00020000);
    ld.rW = __builtin_amdgcn_make_buffer_rsrc((void*)wqp, 0, DM * HD * 2, 0x00020000);
    ld.sX[1] = (unsigned)(bsub * SQ) * (DM * 2);
    ld.sK[1] = (unsigned)((bsub * NH + head) * SK) * (DH * 2);
    const int rb = wg >> 1, nh = wg & 1, b2 = rb >> 6, ks0 = (rb & 63) * 3;
    ld.rWO = __builtin_amdgcn_make_buffer_rsrc((void*)(b2 ? woB : woA), 0, HD * DM * 2, 0x00020000);
    ld.rWF = __builtin_amdgcn_make_buffer_rsrc((void*)WOws, 0, 2 * HD * DM * 2, 0x00020000);
    ld.vWO = l3 * (DM * 2) + l7 * 16;
    ld.vWA = ldsb + SCR_OFF + l3 * 160 + l7 * 16;
    ld.vWR = ldsb + SCR_OFF + (4 * (l >> 4) + ((l >> 2) & 3)) * 160 + (l & 3) * 8;
    ld.vWF = l * 16;
    ld.base_step = 0;
    xw_load<36>(ld);
    xw_load<37>(ld); xw_load<38>(ld); kv_chunk<16, 0>(ld); kv_chunk<16, 1>(ld);
    xw_load<39>(ld); xw_load<40>(ld);
    vm_wait<younger(37, lseq_before(18, 1000))>();
    publish(ld, 2);
    kv_chunk<16, 2>(ld); kv_chunk<17, 0>(ld);
    for (int u = 0; u < 4; ++u) {
      ld.base_step = 19 * u; ld.base_load = 36 * u;
      ld.sX[0] = ld.sX[1]; ld.sK[0] = ld.sK[1];
      ld.sX[1] = (u < 3) ? ld.sX[0] + 4 * SQ * (DM * 2) : 0x40000000u;
      ld.sK[1] = (u < 3) ? ld.sK[0] + 4 * NH * SK * (DH * 2) : 0x40000000u;
      ld.sW[1] = (u < 3) ? ld.sW[0] : 0x40000000u;
      ld.sWO = (u < 3) ? (unsigned)(32 * (ks0 + u)) * (DM * 2) + nh * 640 : 0x40000000u;
      ld.sWF = (u < 3) ? (unsigned)(((b2 * 8 + nh * 4) * 192 + ks0 + u) * 5) * 1024 : 0x40000000u;
      asm volatile("" ::: ACLOB, ACLOB2);
      loader_unit(ld, make_iseq<19>{});
    }
    asm volatile("s_waitcnt vmcnt(0)" ::: "memory");
    return;
  }

  const int cc = l & 15, g = l >> 4, q4 = (l >> 2) & 3, p4 = l & 3;
  St c;
  {
    const unsigned swz8 = (cc >> 1) & 7;
    c.aX0 = ldsb + (48 * wave + cc) * 128 + ((g ^ swz8) << 4);
    c.aX1 = ldsb + (48 * wave + cc) * 128 + (((4 + g) ^ swz8) << 4);
    c.aK0 = ldsb + cc * 384 + ((g ^ swz8) << 4);
    c.aK1 = ldsb + cc * 384 + (((4 + g) ^ swz8) << 4);
    const unsigned fp = ((q4 >> 1) & 1) | ((g & 1) << 1);
#pragma unroll
    for (int i = 0; i < 4; ++i) {
      const unsigned inrow = ((i ^ fp) << 5) + ((p4 >> 1) << 4) + ((p4 & 1) << 3);
      c.aW[i] = ldsb + (8 * g + q4) * 384 + inrow;
      c.aV[i] = ldsb + (4 * g + q4) * 384 + inrow;
    }
    u32x4 o1 = {0x3F803F80u, 0x3F803F80u, 0x3F803F80u, 0x3F803F80u};
    c.ones = __builtin_bit_cast(bf16x8, o1); OPAQUE(c.ones);
    c.pend = false;
    c.aflag = ldsb + FLAG_OFF; c.adone = ldsb + FLAG_OFF + 16 + wave * 4; c.step = 0;
    OPAQUE(c.aflag); OPAQUE(c.adone); OPAQUE(c.step);
    c.voO = l * 16;
    c.rO = __builtin_amdgcn_make_buffer_rsrc((void*)Ows, 0, 0, 0x00020000);
    asm volatile("" ::: ACLOB, ACLOB2);
  }

  flag_read(c); flag_wait(c, 2);
  c.bX0 = vadd_c<slot_of(0)>(c.aX0); c.bX1 = vadd_c<slot_of(0)>(c.aX1);
  c.bW[0] = vadd_c<slot_of(1)>(c.aW[0]); c.bW[1] = vadd_c<slot_of(1)>(c.aW[1]); c.bW[2] = vadd_c<slot_of(1)>(c.aW[2]); c.bW[3] = vadd_c<slot_of(1)>(c.aW[3]);
  read_frag<PH_QP, 0, 0, 0>(c); read_frag<PH_QP, 0, 0, 1>(c);
  read_x<0, 0>(c); read_x<0, 1>(c); read_x<0, 2>(c);
  read_frag<PH_QP, 0, 1, 0>(c); read_frag<PH_QP, 0, 1, 1>(c);
  read_frag<PH_QP, 0, 2, 0>(c); read_frag<PH_QP, 0, 2, 1>(c);
  read_frag<PH_QP, 0, 3, 0>(c); read_frag<PH_QP, 0, 3, 1>(c);
  read_frag<PH_QP, 0, 4, 0>(c); read_frag<PH_QP, 0, 4, 1>(c);

  for (int u = 0; u < 4; ++u) {
    const int batch = 4 * u + bsub;
    const unsigned nb = 36 * u;
    asm volatile("" ::: ACLOB, ACLOB2);
    GAP();
    phase<PH_QP, 0, 0, PH_QP, 0,  2,  3,  4, true,  0, 0, 0>(c, nb);
    phase<PH_QP, 0, 0, PH_QP, 0,  4,  5,  6, false, 0, 1, 1>(c, nb);
    phase<PH_QP, 0, 0, PH_QP, 0,  6,  7,  8, false, 0, 2, 2>(c, nb);
    phase<PH_QP, 0, 0, PH_QP, 0,  8,  9, 10, false, 0, 3, 3>(c, nb);
    phase<PH_QP, 0, 0, PH_QP, 0, 10, 11, 12, false, 0, 4, 4>(c, nb);
    phase<PH_QP, 0, 0, PH_QP, 0, 12, 13, 14, false, 0, 5, 5>(c, nb);
    phase<PH_QP, 0, 0, PH_QP, 0, 14, 15, 16, false, 0, 6, 6>(c, nb);
    phase<PH_QP, 0, 0, PH_QP, 0, 16, 17, 18, false, 0, -1, 7>(c, nb);
    phase<PH_QP, 0, 0, PH_QP, 0, 18, 19, 20, false, 0, -1, 8>(c, nb);
    phase<PH_QP, 0, 0, PH_QK, 0, kload(0), -1, 21, false, 0, -1, 9>(c, nb);
    asm volatile("s_nop 7" ::: "memory");
    GAP();
    q_convert_all(c, make_iseq<18>{});
#pragma unroll
    for (int rt = 0; rt < 3; ++rt) { c.negm4[rt] = f32x4{0.f, 0.f, 0.f, 0.f}; OPAQUE(c.negm4[rt]); }
    GAP();
    asm volatile("s_nop 1" ::: "memory");
    GAP();
    phase<PH_QK, 0, 0, PH_QK, 1, -1, -1, 0, false, 2, -1, 10>(c, nb);
    asm volatile("s_nop 7" ::: "memory");
    GAP();
    sm_ops<0, true, 6, 13>(c);
    GAP();
    asm volatile("s_nop 1" ::: "memory");
    GAP();
    phase<PH_QK, 1, 1, PH_PV, 0, kload(1), vload(0), 23, false, 3, -1, 11>(c, nb);
    phase<PH_PV, 0, 0, PH_QK, 0, -1, -1, 0, true, 1, -1, 12>(c, nb);
    phase<PH_QK, 0, 0, PH_PV, 1, -1, -1, 0, false, 1, -1, 13>(c, nb);
    phase<PH_PV, 1, 1, PH_QK, 1, -1, -1, 0, false, 1, -1, 14>(c, nb);
    phase<PH_QK, 1, 1, PH_PV, 0, kload(2), vload(1), 25, false, 1, -1, 15>(c, nb);
    c.rs0 = slot_of(25);
    for (unsigned nd = nb + 27; nd != nb + 39; nd += 2) {
      const bool lastj = nd == nb + 37;
      c.rs1 = lastj ? c.rs0 : ((c.rs0 == 5 * SLOT) ? 0u : c.rs0 + SLOT);
      phase<PH_PV, 0, 0, PH_QK, 0, -1, -1, 0, false, 1>(c, nb);
      phase<PH_QK, 0, 0, PH_PV, 1, -1, -1, 0, false, 1>(c, nb);
      phase<PH_PV, 1, 1, PH_QK, 1, -1, -1, 0, false, 1>(c, nb);
      phase<PH_QK, 1, 1, PH_PV, 0, -2, -2, 0, false, 1>(c, lastj ? nb + 36 : nd);
      c.rs0 = (c.rs0 == 5 * SLOT) ? SLOT : c.rs0 + 2 * SLOT;
    }
    phase<PH_PV, 0, 0, PH_PV, 1, -1, -1, 0, false, 1, -1, 16>(c, nb);
    sm_rest_<1>(c, make_iseq<SM_NOPS>{});
    GAP();
    asm volatile("s_nop 1" ::: "memory");
    GAP();
    phase<PH_PV, 1, 1, PH_QP, 0, 0, 1, 38, false, 0, -1, 17>(c, nb);
    c.rO = __builtin_amdgcn_make_buffer_rsrc((void*)(Ows + ((size_t)((blk * NB + batch) * 192 + head * 6) * 9 + wave * 3) * 512), 0, 48 * 1024, 0x00020000);
    asm volatile("s_nop 7" ::: "memory");
  }
  GAP();
  ep_ops<true, 0, EP_NOPS>(c);
}

constexpr unsigned LDS2_BYTES = 45 * 3 * 1024;
struct Op {
  u32x4 of[4][9], wf[4][5];
  __amdgpu_buffer_rsrc_t rO, rW;
  unsigned vo[9];
};
template <int ST, int I> DI void op_load(Op& o, unsigned koff) {
  if constexpr (I < 5) o.wf[ST][I] = __builtin_amdgcn_raw_buffer_load_b128(o.rW, o.vo[I], koff * 5, 0);
  else                 o.of[ST][I - 5] = __builtin_amdgcn_raw_buffer_load_b128(o.rO, o.vo[I - 5], koff * 9, 0);
}
template <int ST, bool Z, int M> DI void op_gap(Op& o, unsigned koff_next) {
  constexpr int h = M & 1, m = M >> 1, rt = m / 5, nt = m % 5, R = 4 * m;
  if constexpr (Z && h == 0) asm volatile("v_mfma_f32_16x16x32_bf16 a[%c2:%c3], %0, %1, 0" :: "v"(o.wf[ST][nt]), "v"(o.of[ST][rt]), "i"(R), "i"(R + 3));
  else                       asm volatile("v_mfma_f32_16x16x32_bf16 a[%c2:%c3], %0, %1, a[%c2:%c3]" :: "v"(o.wf[ST + h][nt]), "v"(o.of[ST + h][rt]), "i"(R), "i"(R + 3));
  GAP();
  if constexpr (M % 3 == 1 && M / 3 < 28) {
    constexpr int i = M / 3;
    op_load<(ST + 2 + (i & 1)) % 4, i / 2>(o, koff_next + (i & 1) * 1024);
  }
  GAP();
}
template <int ST, bool Z, int... Ms> DI void op_step_(Op& o, unsigned koff_next, iseq<Ms...>) { (op_gap<ST, Z, Ms>(o, koff_next), ...); }
template <int ST, bool Z> DI void op_step(Op& o, unsigned koff_next) { op_step_<ST, Z>(o, koff_next, make_iseq<90>{}); }
template <int ST, int... Is> DI void op_load_all(Op& o, unsigned koff, iseq<Is...>) { (op_load<ST, Is>(o, koff), ...); }
template <int I> DI f32x4 acc_read4() { f32x4 v; v[0] = acc_read<4 * I>(); v[1] = acc_read<4 * I + 1>(); v[2] = acc_read<4 * I + 2>(); v[3] = acc_read<4 * I + 3>(); return v; }
template <int W, int I> DI void op_red_write(unsigned rb) {
  constexpr int own = I & 3;
  if constexpr (own != W) { constexpr int slot = (W < own) ? W : W - 1; *(LAS f32x4*)(uintptr_t)(rb + (I * 3 + slot) * 1024) = acc_read4<I>(); }
}
template <int W, int I> DI void op_red_final(unsigned rb, uint16_t* outp) {
  if constexpr ((I & 3) == W) {
    f32x4 v = acc_read4<I>();
    v += *(const LAS f32x4*)(uintptr_t)(rb + (I * 3 + 0) * 1024);
    v += *(const LAS f32x4*)(uintptr_t)(rb + (I * 3 + 1) * 1024);
    v += *(const LAS f32x4*)(uintptr_t)(rb + (I * 3 + 2) * 1024);
    constexpr int rt = I / 5, nt = I % 5;
    u32x2 w = {cvtpk(v[0], v[1]), cvtpk(v[2], v[3])};
    *(u32x2*)(outp + (size_t)(rt * 16) * (2 * DM) + nt * 16) = w;
  }
}
template <int W, int... Is> DI void op_finish(unsigned rb, uint16_t* outp, iseq<Is...>) {
  (op_red_write<W, Is>(rb), ...);
  asm volatile("s_waitcnt lgkmcnt(0)\n\ts_barrier" ::: "memory");
  (op_red_final<W, Is>(rb, outp), ...);
}

__global__ __launch_bounds__(256, 1)
void oproj_kernel(const uint16_t* __restrict__ Ows, const uint16_t* __restrict__ WOws, uint16_t* __restrict__ out) {
  extern __shared__ __attribute__((aligned(16))) char smem[];
  const int tid = threadIdx.x;
  const int wave = __builtin_amdgcn_readfirstlane(tid >> 6);
  const int l = tid & 63, c = l & 15, g = l >> 4;
  const int wg = blockIdx.x, xcd = wg & 7, wi = wg >> 3;
  const int blk = xcd >> 2, batch = (xcd & 3) * 4 + (wi >> 3), ng = wi & 7;
  Op o;
  o.rO = __builtin_amdgcn_make_buffer_rsrc((void*)(Ows + ((size_t)((blk * NB + batch) * 192 + wave * 48) * 9) * 512), 0, 48 * 9 * 1024, 0x00020000);
  o.rW = __builtin_amdgcn_make_buffer_rsrc((void*)(WOws + ((size_t)((blk * 8 + ng) * 192 + wave * 48) * 5) * 512), 0, 48 * 5 * 1024, 0x00020000);
#pragma unroll
  for (int i = 0; i < 9; ++i) { o.vo[i] = l * 16 + i * 1024; OPAQUE(o.vo[i]); }
  asm volatile("" ::: ACLOB, "a156","a157","a158","a159","a160","a161","a162","a163","a164","a165","a166","a167","a168","a169","a170","a171","a172","a173","a174","a175","a176","a177","a178","a179");
  op_load_all<0>(o, 0, make_iseq<14>{});
  op_load_all<1>(o, 1024, make_iseq<14>{});
  GAP();
  op_step<0, true>(o, 2 * 1024);
  op_step<2, false>(o, 4 * 1024);
  for (int ks = 4; ks < 48; ks += 4) {
    op_step<0, false>(o, (ks + 2) * 1024);
    op_step<2, false>(o, (ks + 4) * 1024);
  }
  asm volatile("s_nop 7" ::: "memory");
  GAP();
  const unsigned rb = (unsigned)(uintptr_t)(LAS char*)smem + l * 16;
  uint16_t* outp = out + (size_t)(batch * SQ + c) * (2 * DM) + blk * DM + ng * 80 + 4 * g;
  if (wave == 0)      op_finish<0>(rb, outp, make_iseq<45>{});
  else if (wave == 1) op_finish<1>(rb, outp, make_iseq<45>{});
  else if (wave == 2) op_finish<2>(rb, outp, make_iseq<45>{});
  else                op_finish<3>(rb, outp, make_iseq<45>{});
}

extern "C" void kernel_launch(void* const* d_in, const int* in_sizes, int n_in,
                              void* d_out, int out_size, void* d_ws, size_t ws_size,
                              hipStream_t stream) {
  (void)in_sizes; (void)n_in; (void)out_size; (void)ws_size;
  const uint16_t* xA = (const uint16_t*)d_in[0]; const uint16_t* kA = (const uint16_t*)d_in[1];
  const uint16_t* vA = (const uint16_t*)d_in[2]; const uint16_t* wqA = (const uint16_t*)d_in[3];
  const uint16_t* woA = (const uint16_t*)d_in[4];
  const uint16_t* xB = (const uint16_t*)d_in[5]; const uint16_t* kB = (const uint16_t*)d_in[6];
  const uint16_t* vB = (const uint16_t*)d_in[7]; const uint16_t* wqB = (const uint16_t*)d_in[8];
  const uint16_t* woB = (const uint16_t*)d_in[9];
  uint16_t* Ows = (uint16_t*)d_ws;
  uint16_t* WOws = (uint16_t*)((char*)d_ws + WS_WO_OFF);
  (void)hipFuncSetAttribute((const void*)attn_fused_kernel, hipFuncAttributeMaxDynamicSharedMemorySize, (int)LDS1_BYTES);
  (void)hipFuncSetAttribute((const void*)oproj_kernel, hipFuncAttributeMaxDynamicSharedMemorySize, (int)LDS2_BYTES);
  hipLaunchKernelGGL(attn_fused_kernel, dim3(256), dim3(256), LDS1_BYTES, stream,
                     xA, kA, vA, wqA, woA, xB, kB, vB, wqB, woB, Ows, WOws);
  hipLaunchKernelGGL(oproj_kernel, dim3(256), dim3(256), LDS2_BYTES, stream, Ows, WOws, (uint16_t*)d_out);
}
```
